```python
import torch

def _softmax(x, axis=-1):
    return torch.softmax(x, dim=axis)
def _gelu(x):
    return torch.nn.functional.gelu(x, approximate="tanh")
def _silu(x):
    return torch.nn.functional.silu(x)
def _sigmoid(x):
    return torch.sigmoid(x)
def _layernorm(x, axis=-1, eps=1e-5):
    m = x.mean(dim=axis, keepdim=True)
    v = x.var(dim=axis, keepdim=True, unbiased=False)
    return (x - m) / torch.sqrt(v + eps)
def _scatter_add(tgt, idx, src, axis=0):
    return tgt.clone().index_add_(axis, idx, src)
def _segment_sum(src, seg, n):
    out = torch.zeros((n,) + src.shape[1:], dtype=src.dtype)
    return out.index_add_(0, seg, src)
def _pad(x, pw):
    flat = tuple(v for pair in reversed(pw) for v in pair)
    return torch.nn.functional.pad(x, flat)

kernel_name = 'sequential_preLN__linear_attn_block__swiglu_ffn__prompt'

def setup_inputs():

    _w = (lambda a: (lambda t: t.to(torch.bfloat16) if t.is_floating_point() else t)(torch.as_tensor(a)))
    def _A_setup():
        B, S, D = 1, 2048, 512
        return {
            "x": _w((lambda *s: torch.randn(*s, dtype=torch.bfloat16) * 0.05)(B, S, D)),
            "wqkv": _w((lambda *s: torch.randn(*s, dtype=torch.bfloat16) * 0.05)(D, 3 * D)),
            "wo": _w((lambda *s: torch.randn(*s, dtype=torch.bfloat16) * 0.05)(D, D)),
        }
    _A_in = _A_setup()

    def _B_setup():
        B, S, D, F = 1, 2048, 512, 1792
        return {
            "x": _w((lambda *s: torch.randn(*s, dtype=torch.bfloat16) * 0.05)(B, S, D)),
            "w_gate": _w((lambda *s: torch.randn(*s, dtype=torch.bfloat16) * 0.05)(D, F)),
            "w_up": _w((lambda *s: torch.randn(*s, dtype=torch.bfloat16) * 0.05)(D, F)),
            "w_down": _w((lambda *s: torch.randn(*s, dtype=torch.bfloat16) * 0.05)(F, D)),
        }
    _B_in = _B_setup()

    return {
        "A_x": _A_in["x"],
        "A_wqkv": _A_in["wqkv"],
        "A_wo": _A_in["wo"],
        "B_x": _B_in["x"],
        "B_w_gate": _B_in["w_gate"],
        "B_w_up": _B_in["w_up"],
        "B_w_down": _B_in["w_down"],
    }

def reference(A_x, A_wqkv, A_wo, B_x, B_w_gate, B_w_up, B_w_down):
    def _A_ref(x, wqkv, wo):
        B, S, D = x.shape
        H = 32; Dh = D // H
        qkv = (lambda x, *d: x.permute(*d))((x @ wqkv).reshape(B, S, 3, H, Dh), 2, 0, 3, 1, 4)
        q, k, v = qkv[0], qkv[1], qkv[2]
        phi = lambda t: torch.where(t > 0, t + 1, torch.exp(t)) + 1e-6
        pq, pk = phi(q), phi(k)
        kv = torch.einsum("bhsd,bhse->bhde", pk, v)
        z = torch.einsum("bhsd->bhd", pk)
        num = torch.einsum("bhqd,bhde->bhqe", pq, kv)
        den = torch.einsum("bhqd,bhd->bhq", pq, z)[..., None] + 1e-6
        o = num / den
        return (lambda x, *d: x.permute(*d))(o, 0, 2, 1, 3).reshape(B, S, D) @ wo

    def _B_ref(x, w_gate, w_up, w_down):
        g = _silu(x @ w_gate)
        u = x @ w_up
        return (g * u) @ w_down

    x = A_x
    h = x + _A_ref(_layernorm(x, axis=-1), A_wqkv, A_wo)
    h2 = h + _B_ref(_layernorm(h, axis=-1), B_w_gate, B_w_up, B_w_down)
    return h2
```

```cpp
#include <hip/hip_runtime.h>
#include <hip/hip_bf16.h>
typedef unsigned short u16;
__device__ __forceinline__ float b2f(u16 b){ return __uint_as_float(((unsigned)b)<<16); }
__device__ __forceinline__ u16 f2b(float f){ unsigned u=__float_as_uint(f); u += 0x7FFFu + ((u>>16)&1u); return (u16)(u>>16); }
constexpr int S=2048,D=512,F=1792,H=32,DH=16;

__global__ void k_ln(const u16* x, u16* y){
  int row=blockIdx.x, t=threadIdx.x;
  float v[8]; float s=0;
  for(int i=0;i<8;i++){ v[i]=b2f(x[row*D+t*8+i]); s+=v[i]; }
  for(int o=32;o;o>>=1) s+=__shfl_xor(s,o);
  float m=s/D; float q=0;
  for(int i=0;i<8;i++){ float d=v[i]-m; q+=d*d; }
  for(int o=32;o;o>>=1) q+=__shfl_xor(q,o);
  float r=rsqrtf(q/D+1e-5f);
  for(int i=0;i<8;i++) y[row*D+t*8+i]=f2b((v[i]-m)*r);
}
template<int MODE>
__global__ void k_gemm(const u16* A,const u16* B,u16* C,const u16* res,int M,int N,int K){
  __shared__ float As[16][17], Bs[16][17];
  int tx=threadIdx.x&15, ty=threadIdx.x>>4;
  int row=blockIdx.y*16+ty, col=blockIdx.x*16+tx;
  float acc=0;
  for(int k0=0;k0<K;k0+=16){
    As[ty][tx]=b2f(A[row*K+k0+tx]); Bs[ty][tx]=b2f(B[(k0+ty)*N+col]);
    __syncthreads();
    for(int k=0;k<16;k++) acc+=As[ty][k]*Bs[k][tx];
    __syncthreads();
  }
  if(MODE==1) acc = b2f(f2b(acc)) + b2f(res[row*N+col]);
  C[row*N+col]=f2b(acc);
}
__device__ __forceinline__ float phi(float t){ return (t>0? t+1.f : __expf(t)) + 1e-6f; }
__global__ void k_kv(const u16* qkv, float* kv, float* z){
  int h=blockIdx.x, t=threadIdx.x; int d=t>>4,e=t&15; float a=0,zz=0;
  for(int s=0;s<S;s++){ float pk=b2f(f2b(phi(b2f(qkv[s*1536+512+h*16+d])))); a+=pk*b2f(qkv[s*1536+1024+h*16+e]); zz+=pk; }
  kv[h*256+t]=b2f(f2b(a)); if(e==0) z[h*16+d]=b2f(f2b(zz));
}
__global__ void k_o(const u16* qkv,const float* kv,const float* z,u16* o){
  int s=blockIdx.x, t=threadIdx.x; int h=t>>4,e=t&15; float n=0,dn=0;
  for(int d=0;d<16;d++){ float pq=b2f(f2b(phi(b2f(qkv[s*1536+h*16+d])))); n+=pq*kv[h*256+d*16+e]; dn+=pq*z[h*16+d]; }
  o[s*D+t]=f2b(n/(dn+1e-6f));
}
__global__ void k_act(const u16* g,const u16* u,u16* a){
  int i=blockIdx.x*256+threadIdx.x; float gg=b2f(g[i]); float s=gg/(1.f+__expf(-gg)); a[i]=f2b(b2f(f2b(s))*b2f(u[i]));
}
extern "C" void kernel_launch(void* const* d_in, const int* in_sizes, int n_in, void* d_out, int out_size, void* d_ws, size_t ws_size, hipStream_t stream){
  const u16* x=(const u16*)d_in[0]; const u16* wqkv=(const u16*)d_in[1]; const u16* wo=(const u16*)d_in[2];
  const u16* wg=(const u16*)d_in[4]; const u16* wu=(const u16*)d_in[5]; const u16* wd=(const u16*)d_in[6];
  char* w=(char*)d_ws;
  u16* xn=(u16*)w; w+=S*D*2; u16* qkv=(u16*)w; w+=S*1536*2; float* kv=(float*)w; w+=H*256*4; float* z=(float*)w; w+=H*16*4;
  u16* o=(u16*)w; w+=S*D*2; u16* h=(u16*)w; w+=S*D*2; u16* hn=(u16*)w; w+=S*D*2; u16* g=(u16*)w; w+=S*F*2; u16* u=(u16*)w; w+=S*F*2; u16* a=(u16*)w; w+=S*F*2;
  k_ln<<<S,64,0,stream>>>(x,xn);
  k_gemm<0><<<dim3(1536/16,S/16),256,0,stream>>>(xn,wqkv,qkv,nullptr,S,1536,D);
  k_kv<<<H,256,0,stream>>>(qkv,kv,z);
  k_o<<<S,512,0,stream>>>(qkv,kv,z,o);
  k_gemm<1><<<dim3(D/16,S/16),256,0,stream>>>(o,wo,h,x,S,D,D);
  k_ln<<<S,64,0,stream>>>(h,hn);
  k_gemm<0><<<dim3(F/16,S/16),256,0,stream>>>(hn,wg,g,nullptr,S,F,D);
  k_gemm<0><<<dim3(F/16,S/16),256,0,stream>>>(hn,wu,u,nullptr,S,F,D);
  k_act<<<S*F/256,256,0,stream>>>(g,u,a);
  k_gemm<1><<<dim3(D/16,S/16),256,0,stream>>>(a,wd,(u16*)d_out,h,S,D,F);
}
```

```cpp
#include <hip/hip_runtime.h>
#include <hip/hip_bf16.h>
__global__ __launch_bounds__(256, 1) void fused_block_kernel(const void* x, const void* wqkv, const void* wo, const void* wg, const void* wu, const void* wd, void* out, void* ws) {
  __shared__ __attribute__((aligned(16))) char lds[163840];
  const unsigned long long ka = (unsigned long long)__builtin_amdgcn_kernarg_segment_ptr();
  asm volatile(
"s_load_dwordx16 s[80:95], %0, 0x0\n"
"s_mov_b32 s46, %1\n"
"s_getpc_b64 s[68:69]\n"
".LK_pc:\n"
"v_lshrrev_b32 v2, 5, %2\n"
"s_lshr_b32 s70, s46, 3\n"
"v_lshlrev_b32 v14, 7, v2\n"
"s_lshl_b32 s70, s70, 10\n"
"s_mov_b32 s71, .LK_codeend-.LK_pc\n"
"v_add_u32 v14, s70, v14\n"
"v_min_u32 v14, s71, v14\n"
"global_load_dword v254, v14, s[68:69]\n"
"v_and_b32 v1, 63, %2\n"
"v_lshrrev_b32 v2, 6, %2\n"
"s_nop 0\n"
"v_readfirstlane_b32 s45, v2\n"
"s_and_b32 s67, s46, 7\n"
"s_lshr_b32 s68, s46, 3\n"
"s_lshr_b32 s69, s67, 1\n"
"s_and_b32 s67, s67, 1\n"
"s_and_b32 s52, s68, 7\n"
"s_lshr_b32 s70, s68, 3\n"
"s_lshl_b32 s71, s69, 3\n"
"s_add_u32 s47, s71, s52\n"
"s_lshl_b32 s72, s67, 2\n"
"s_add_u32 s48, s72, s70\n"
"s_lshl_b32 s72, s70, 1\n"
"s_add_u32 s72, s72, s67\n"
"s_add_u32 s51, s71, s72\n"
"s_and_b32 s72, s68, 1\n"
"s_lshl_b32 s73, s48, 1\n"
"s_add_u32 s49, s73, s72\n"
"s_lshr_b32 s50, s47, 1\n"
"v_lshrrev_b32 v2, 3, v1\n"
"v_and_b32 v3, 7, v1\n"
"v_lshrrev_b32 v4, 4, v1\n"
"v_lshrrev_b32 v5, 5, v1\n"
"s_lshl_b32 s67, s45, 4\n"
"v_add_u32 v6, s67, v2\n"
"v_xor_b32 v11, v3, v4\n"
"v_lshlrev_b32 v11, 4, v11\n"
"v_lshl_add_u32 v7, v6, 10, v11\n"
"v_xor_b32 v11, 64, v11\n"
"v_lshl_add_u32 v8, v6, 10, v11\n"
"v_add_u32 v8, 7168, v8\n"
"v_and_b32 v11, 1, v4\n"
"v_lshlrev_b32 v11, 2, v11\n"
"v_xor_b32 v11, v3, v11\n"
"v_lshlrev_b32 v11, 4, v11\n"
"v_mul_u32_u24 v9, 3072, v6\n"
"v_add_u32 v9, v9, v11\n"
"v_add_u32 v10, 23552, v9\n"
"v_and_b32 v186, 31, v1\n"
"v_lshlrev_b32 v13, 5, v186\n"
"v_lshl_add_u32 v13, v5, 10, v13\n"
"s_lshl_b32 s5, s47, 16\n"
"s_lshl_b32 s75, s45, 11\n"
"s_lshl_b32 s6, s48, 7\n"
"s_lshl_b32 s67, s48, 1\n"
"s_add_u32 s67, s67, s47\n"
"s_and_b32 s67, s67, 2\n"
"s_mul_i32 s68, s46, 1\n"
"s_add_u32 s67, s67, s68\n"
"s_and_b32 s4, s67, 7\n"
"s_lshl_b32 s67, s48, 13\n"
"s_add_u32 s67, s67, s75\n"
"s_add_u32 s67, s67, s5\n"
"s_mov_b32 s22, 0x200000\n"
"s_mov_b32 s23, 0x20000\n"
"s_mov_b32 s26, 0x180000\n"
"s_mov_b32 s27, 0x20000\n"
"v_bfe_u32 v187, v1, 2, 2\n"
"v_and_b32 v188, 1, v4\n"
"v_and_b32 v189, 3, v1\n"
"s_lshr_b32 s68, s45, 1\n"
"s_lshl_b32 s68, s68, 12\n"
"v_lshlrev_b32 v12, 7, v186\n"
"v_add_u32 v12, s68, v12\n"
"v_bfe_u32 v14, v186, 1, 3\n"
"v_xor_b32 v14, v14, v5\n"
"v_xor_b32 v176, 0, v14\n"
"v_lshl_add_u32 v176, v176, 4, v12\n"
"v_xor_b32 v177, 2, v14\n"
"v_lshl_add_u32 v177, v177, 4, v12\n"
"v_xor_b32 v178, 4, v14\n"
"v_lshl_add_u32 v178, v178, 4, v12\n"
"v_xor_b32 v179, 6, v14\n"
"v_lshl_add_u32 v179, v179, 4, v12\n"
"v_lshlrev_b32 v190, 5, v188\n"
"v_lshl_add_u32 v190, v189, 3, v190\n"
"v_lshrrev_b32 v191, 1, v187\n"
"s_and_b32 s68, s45, 1\n"
"v_xor_b32 v12, s68, v191\n"
"v_lshl_add_u32 v12, v12, 6, v190\n"
"v_lshl_add_u32 v14, v5, 3, v187\n"
"v_lshl_add_u32 v180, v14, 7, v12\n"
"s_waitcnt lgkmcnt(0)\n"
"s_mov_b32 s20, s80\n"
"s_and_b32 s21, s81, 0xffff\n"
"s_mov_b32 s24, s82\n"
"s_and_b32 s25, s83, 0xffff\n"
"s_load_dword s44, s[94:95], 0x0\n"
"s_add_u32 m0, s75, 0\n"
"s_lshl_b32 s63, s4, 7\n"
"s_add_u32 s63, s63, s5\n"
"buffer_load_dwordx4 v7, s[20:23], s63 offen lds\n"
"buffer_load_dwordx4 v8, s[20:23], s63 offen offset:1024 lds\n"
"s_mul_i32 s7, s4, 196608\n"
"s_add_u32 s7, s7, s6\n"
"s_add_u32 m0, s75, 8192\n"
"s_add_u32 s64, s7, 1024\n"
"buffer_load_dwordx4 v9, s[24:27], s64 offen lds\n"
"buffer_load_dwordx4 v10, s[24:27], s64 offen offset:1024 lds\n"
"s_add_u32 m0, s75, 16384\n"
"s_add_u32 s65, s7, 2048\n"
"buffer_load_dwordx4 v9, s[24:27], s65 offen lds\n"
"buffer_load_dwordx4 v10, s[24:27], s65 offen offset:1024 lds\n"
"s_add_u32 s4, s4, 1\n"
"s_and_b32 s4, s4, 7\n"
"buffer_load_dwordx4 v[144:147], v13, s[20:23], s67 offen\n"
"buffer_load_dwordx4 v[148:151], v13, s[20:23], s67 offen offset:16\n"
"s_mov_b32 s16, s94\n"
"s_and_b32 s17, s95, 0xffff\n"
"s_mov_b32 s18, 0x1520000\n"
"s_mov_b32 s19, 0x20000\n"
"s_mov_b32 s32, s86\n"
"s_and_b32 s33, s87, 0xffff\n"
"s_add_u32 m0, s75, 24576\n"
"s_lshl_b32 s63, s4, 7\n"
"s_add_u32 s63, s63, s5\n"
"buffer_load_dwordx4 v7, s[20:23], s63 offen lds\n"
"s_mov_b32 s34, 0x1c0000\n"
"s_mov_b32 s35, 0x20000\n"
"s_mov_b32 s36, s88\n"
"s_and_b32 s37, s89, 0xffff\n"
"s_mov_b32 s38, 0x1c0000\n"
"s_mov_b32 s39, 0x20000\n"
"buffer_load_dwordx4 v8, s[20:23], s63 offen offset:1024 lds\n"
"s_mul_i32 s7, s4, 196608\n"
"s_add_u32 s7, s7, s6\n"
"s_mov_b32 s40, s90\n"
"s_and_b32 s41, s91, 0xffff\n"
"s_mov_b32 s42, 0x1c0000\n"
"s_mov_b32 s43, 0x20000\n"
"s_mov_b32 s28, s84\n"
"s_and_b32 s29, s85, 0xffff\n"
"s_add_u32 m0, s75, 32768\n"
"s_add_u32 s64, s7, 1024\n"
"buffer_load_dwordx4 v9, s[24:27], s64 offen lds\n"
"s_mov_b32 s30, 0x80000\n"
"s_mov_b32 s31, 0x20000\n"
"s_mov_b64 s[78:79], s[92:93]\n"
"s_mov_b32 s54, 0x358637bd\n"
"s_mov_b32 s55, 0x358637bd\n"
"s_mov_b32 s56, 0x3fb8aa3b\n"
"buffer_load_dwordx4 v10, s[24:27], s64 offen offset:1024 lds\n"
"s_mov_b32 s57, 0x3fb8aa3b\n"
"s_mov_b32 s58, 0xffff\n"
"s_mov_b32 s59, 0xffff\n"
"s_mov_b32 s60, 0\n"
"s_mov_b32 s61, -1\n"
"s_mov_b32 s62, 0x3f803f80\n"
"s_add_u32 m0, s75, 40960\n"
"s_add_u32 s65, s7, 2048\n"
"buffer_load_dwordx4 v9, s[24:27], s65 offen lds\n"
"v_add_u32 v181, 49152, v176\n"
"v_add_u32 v182, 49152, v177\n"
"v_add_u32 v183, 49152, v178\n"
"v_add_u32 v184, 49152, v179\n"
"v_add_u32 v185, 49152, v180\n"
"v_bfe_u32 v12, v1, 2, 1\n"
"buffer_load_dwordx4 v10, s[24:27], s65 offen offset:1024 lds\n"
"s_add_u32 s4, s4, 1\n"
"s_and_b32 s4, s4, 7\n"
"v_cmp_eq_u32 vcc, v12, v188\n"
"v_mov_b32 v12, s62\n"
"v_lshlrev_b32 v6, 4, v1\n"
"v_mov_b32 v250, v9\n"
"v_cndmask_b32 v192, 0, v12, vcc\n"
"v_cndmask_b32 v193, 0, v12, vcc\n"
"v_cndmask_b32 v194, 0, v12, vcc\n"
"v_cndmask_b32 v195, 0, v12, vcc\n"
"s_lshr_b32 s67, s48, 1\n"
"s_mul_i32 s67, s67, 7\n"
"s_and_b32 s68, s48, 1\n"
"s_mul_i32 s68, s68, 3\n"
"s_add_u32 s67, s67, s68\n"
"s_lshr_b32 s68, s47, 3\n"
"s_add_u32 s72, s67, s68\n"
"s_and_b32 s68, s47, 7\n"
"s_lshl_b32 s68, s68, 2\n"
"s_add_u32 s73, s68, s45\n"
"s_mul_i32 s8, s73, 57344\n"
"s_lshl_b32 s68, s72, 7\n"
"s_add_u32 s8, s8, s68\n"
"v_mul_u32_u24 v196, 3584, v2\n"
"v_lshl_add_u32 v196, v3, 4, v196\n"
"v_add_u32 v197, 27648, v196\n"
"s_mul_i32 s77, s45, 6144\n"
"s_add_u32 s77, s77, 139264\n"
"s_lshl_b32 s67, s47, 2\n"
"s_add_u32 s74, s67, s45\n"
"s_min_u32 s67, s74, 111\n"
"s_mul_i32 s68, s67, 2341\n"
"s_lshr_b32 s68, s68, 16\n"
"s_mul_i32 s68, s68, 28\n"
"s_sub_u32 s67, s67, s68\n"
"s_cmp_lt_u32 s67, 8\n"
"s_cselect_b32 s74, 112, s74\n"
"s_and_b32 s69, s67, 3\n"
"s_mul_i32 s69, s69, 7\n"
"s_lshr_b32 s67, s67, 2\n"
"s_add_u32 s67, s67, s69\n"
"s_add_u32 s67, s67, s68\n"
"s_lshl_b32 s67, s67, 14\n"
"s_add_u32 s9, s67, s6\n"
"s_cmp_lt_u32 s74, 112\n"
"s_cselect_b32 s9, s9, 1073741824\n"
"v_lshlrev_b32 v198, 10, v2\n"
"v_lshl_add_u32 v198, v3, 4, v198\n"
"v_add_u32 v199, 7168, v198\n"
"s_waitcnt vmcnt(8)\n"
"s_barrier\n"
"ds_read_b128 a[128:131], v176 offset:0\n"
"ds_read_b64_tr_b16 v[76:77], v180 offset:8192\n"
"ds_read_b64_tr_b16 v[78:79], v180 offset:8704\n"
"ds_read_b64_tr_b16 v[80:81], v180 offset:16384\n"
"ds_read_b64_tr_b16 v[82:83], v180 offset:16896\n"
"ds_read_b128 a[132:135], v177 offset:0\n"
"ds_read_b64_tr_b16 v[84:85], v180 offset:10240\n"
"ds_read_b64_tr_b16 v[86:87], v180 offset:10752\n"
"ds_read_b64_tr_b16 v[88:89], v180 offset:18432\n"
"ds_read_b64_tr_b16 v[90:91], v180 offset:18944\n"
"s_waitcnt lgkmcnt(0)\n"
"v_mfma_f32_32x32x16_bf16 v[32:47], a[128:131], v[76:79], 0\n"
"ds_read_b128 a[136:139], v178 offset:0\n"
"ds_read_b64_tr_b16 v[92:93], v180 offset:12288\n"
"ds_read_b64_tr_b16 v[94:95], v180 offset:12800\n"
"v_mfma_f32_32x32x16_bf16 v[48:63], a[128:131], v[80:83], 0\n"
"ds_read_b64_tr_b16 v[96:97], v180 offset:20480\n"
"ds_read_b64_tr_b16 v[98:99], v180 offset:20992\n"
"s_add_u32 m0, s75, 49152\n"
"s_lshl_b32 s63, s4, 7\n"
"s_add_u32 s63, s63, s5\n"
"buffer_load_dwordx4 v7, s[20:23], s63 offen lds\n"
"v_mfma_f32_16x16x32_bf16 v[68:71], v[192:195], v[76:79], 0\n"
"buffer_load_dwordx4 v8, s[20:23], s63 offen offset:1024 lds\n"
"s_mul_i32 s7, s4, 196608\n"
"s_add_u32 s7, s7, s6\n"
"v_mfma_f32_16x16x32_bf16 v[72:75], v[192:195], v[80:83], 0\n"
"s_add_u32 m0, s75, 57344\n"
"s_add_u32 s64, s7, 1024\n"
"buffer_load_dwordx4 v9, s[24:27], s64 offen lds\n"
"v_mfma_f32_32x32x16_bf16 v[32:47], a[132:135], v[84:87], v[32:47]\n"
"ds_read_b128 a[140:143], v179 offset:0\n"
"ds_read_b64_tr_b16 v[76:77], v180 offset:14336\n"
"ds_read_b64_tr_b16 v[78:79], v180 offset:14848\n"
"v_mfma_f32_32x32x16_bf16 v[48:63], a[132:135], v[88:91], v[48:63]\n"
"ds_read_b64_tr_b16 v[80:81], v180 offset:22528\n"
"ds_read_b64_tr_b16 v[82:83], v180 offset:23040\n"
"buffer_load_dwordx4 v10, s[24:27], s64 offen offset:1024 lds\n"
"v_mfma_f32_16x16x32_bf16 v[68:71], v[192:195], v[84:87], v[68:71]\n"
"s_add_u32 m0, s75, 65536\n"
"s_add_u32 s65, s7, 2048\n"
"buffer_load_dwordx4 v9, s[24:27], s65 offen lds\n"
"v_mfma_f32_16x16x32_bf16 v[72:75], v[192:195], v[88:91], v[72:75]\n"
"buffer_load_dwordx4 v10, s[24:27], s65 offen offset:1024 lds\n"
"s_add_u32 s4, s4, 1\n"
"s_and_b32 s4, s4, 7\n"
"v_mov_b32 v12, 0\n"
"v_mov_b32 v13, 0\n"
"s_waitcnt vmcnt(13)\n"
"v_dot2_f32_bf16 v12, v144, s62, v12\n"
"v_dot2_f32_bf16 v13, v144, v144, v13\n"
"v_dot2_f32_bf16 v12, v145, s62, v12\n"
"v_dot2_f32_bf16 v13, v145, v145, v13\n"
"v_dot2_f32_bf16 v12, v146, s62, v12\n"
"v_dot2_f32_bf16 v13, v146, v146, v13\n"
"v_dot2_f32_bf16 v12, v147, s62, v12\n"
"v_dot2_f32_bf16 v13, v147, v147, v13\n"
"s_waitcnt vmcnt(12)\n"
"v_dot2_f32_bf16 v12, v148, s62, v12\n"
"v_dot2_f32_bf16 v13, v148, v148, v13\n"
"v_dot2_f32_bf16 v12, v149, s62, v12\n"
"v_dot2_f32_bf16 v13, v149, v149, v13\n"
"v_dot2_f32_bf16 v12, v150, s62, v12\n"
"v_dot2_f32_bf16 v13, v150, v150, v13\n"
"v_dot2_f32_bf16 v12, v151, s62, v12\n"
"v_dot2_f32_bf16 v13, v151, v151, v13\n"
"s_nop 2\n"
"v_add_f32_dpp v12, v12, v12 quad_perm:[1,0,3,2] row_mask:0xf bank_mask:0xf\n"
"v_add_f32_dpp v13, v13, v13 quad_perm:[1,0,3,2] row_mask:0xf bank_mask:0xf\n"
"s_nop 0\n"
"v_add_f32_dpp v12, v12, v12 quad_perm:[2,3,0,1] row_mask:0xf bank_mask:0xf\n"
"v_add_f32_dpp v13, v13, v13 quad_perm:[2,3,0,1] row_mask:0xf bank_mask:0xf\n"
"s_nop 0\n"
"v_add_f32_dpp v12, v12, v12 row_half_mirror row_mask:0xf bank_mask:0xf\n"
"v_add_f32_dpp v13, v13, v13 row_half_mirror row_mask:0xf bank_mask:0xf\n"
"s_nop 0\n"
"v_add_f32_dpp v12, v12, v12 row_mirror row_mask:0xf bank_mask:0xf\n"
"v_add_f32_dpp v13, v13, v13 row_mirror row_mask:0xf bank_mask:0xf\n"
"v_mov_b32 v14, v12\n"
"v_mov_b32 v15, v13\n"
"s_nop 0\n"
"v_permlane16_swap_b32 v12, v14\n"
"v_permlane16_swap_b32 v13, v15\n"
"v_add_f32 v12, v12, v14\n"
"v_add_f32 v13, v13, v15\n"
"v_mul_f32 v12, 0x3b000000, v12\n"
"v_mul_f32 v13, 0x3b000000, v13\n"
"v_fma_f32 v14, -v12, v12, v13\n"
"v_max_f32 v14, 0, v14\n"
"v_add_f32 v14, 0x3727c5ac, v14\n"
"v_rsq_f32 v144, v14\n"
"s_add_u32 s44, s44, 1\n"
"v_mul_f32 v146, v12, v144\n"
"v_sub_f32 v146, 0, v146\n"
"v_mov_b32 v145, s44\n"
"v_mov_b32 v147, s44\n"
"s_lshl_b32 s67, s47, 10\n"
"s_add_u32 s67, s67, s6\n"
"s_lshl_b32 s68, s45, 5\n"
"s_add_u32 s67, s67, s68\n"
"s_add_u32 s67, s67, 4096\n"
"v_lshlrev_b32 v12, 4, v5\n"
"s_mov_b32 exec_lo, 1\n"
"s_mov_b32 exec_hi, 1\n"
"buffer_store_dwordx4 v[144:147], v12, s[16:19], s67 offen sc1\n"
"s_mov_b64 exec, -1\n"
"s_waitcnt vmcnt(7)\n"
"s_waitcnt lgkmcnt(0)\n"
"s_barrier\n"
"v_mfma_f32_32x32x16_bf16 v[32:47], a[136:139], v[92:95], v[32:47]\n"
"ds_read_b128 a[144:147], v176 offset:24576\n"
"ds_read_b64_tr_b16 v[84:85], v180 offset:32768\n"
"ds_read_b64_tr_b16 v[86:87], v180 offset:33280\n"
"v_mfma_f32_32x32x16_bf16 v[48:63], a[136:139], v[96:99], v[48:63]\n"
"ds_read_b64_tr_b16 v[88:89], v180 offset:40960\n"
"ds_read_b64_tr_b16 v[90:91], v180 offset:41472\n"
"s_add_u32 m0, s75, 0\n"
"s_lshl_b32 s63, s4, 7\n"
"s_add_u32 s63, s63, s5\n"
"buffer_load_dwordx4 v7, s[20:23], s63 offen lds\n"
"v_mfma_f32_16x16x32_bf16 v[68:71], v[192:195], v[92:95], v[68:71]\n"
"v_mfma_f32_16x16x32_bf16 v[72:75], v[192:195], v[96:99], v[72:75]\n"
"buffer_load_dwordx4 v8, s[20:23], s63 offen offset:1024 lds\n"
"s_mul_i32 s7, s4, 196608\n"
"s_add_u32 s7, s7, s6\n"
"v_mfma_f32_32x32x16_bf16 v[32:47], a[140:143], v[76:79], v[32:47]\n"
"ds_read_b128 a[148:151], v177 offset:24576\n"
"ds_read_b64_tr_b16 v[92:93], v180 offset:34816\n"
"ds_read_b64_tr_b16 v[94:95], v180 offset:35328\n"
"v_mfma_f32_32x32x16_bf16 v[48:63], a[140:143], v[80:83], v[48:63]\n"
"ds_read_b64_tr_b16 v[96:97], v180 offset:43008\n"
"ds_read_b64_tr_b16 v[98:99], v180 offset:43520\n"
"v_mfma_f32_16x16x32_bf16 v[68:71], v[192:195], v[76:79], v[68:71]\n"
"s_add_u32 m0, s75, 8192\n"
"s_add_u32 s64, s7, 1024\n"
"buffer_load_dwordx4 v9, s[24:27], s64 offen lds\n"
"v_mfma_f32_16x16x32_bf16 v[72:75], v[192:195], v[80:83], v[72:75]\n"
"s_waitcnt lgkmcnt(7)\n"
"v_mfma_f32_32x32x16_bf16 v[32:47], a[144:147], v[84:87], v[32:47]\n"
"ds_read_b128 a[152:155], v178 offset:24576\n"
"ds_read_b64_tr_b16 v[76:77], v180 offset:36864\n"
"ds_read_b64_tr_b16 v[78:79], v180 offset:37376\n"
"s_waitcnt lgkmcnt(8)\n"
"v_mfma_f32_32x32x16_bf16 v[48:63], a[144:147], v[88:91], v[48:63]\n"
"ds_read_b64_tr_b16 v[80:81], v180 offset:45056\n"
"ds_read_b64_tr_b16 v[82:83], v180 offset:45568\n"
"buffer_load_dwordx4 v10, s[24:27], s64 offen offset:1024 lds\n"
"v_mfma_f32_16x16x32_bf16 v[68:71], v[192:195], v[84:87], v[68:71]\n"
"v_mfma_f32_16x16x32_bf16 v[72:75], v[192:195], v[88:91], v[72:75]\n"
"s_add_u32 m0, s75, 16384\n"
"s_add_u32 s65, s7, 2048\n"
"buffer_load_dwordx4 v9, s[24:27], s65 offen lds\n"
"s_waitcnt lgkmcnt(7)\n"
"v_mfma_f32_32x32x16_bf16 v[32:47], a[148:151], v[92:95], v[32:47]\n"
"ds_read_b128 a[156:159], v179 offset:24576\n"
"ds_read_b64_tr_b16 v[84:85], v180 offset:38912\n"
"ds_read_b64_tr_b16 v[86:87], v180 offset:39424\n"
"s_waitcnt lgkmcnt(8)\n"
"v_mfma_f32_32x32x16_bf16 v[48:63], a[148:151], v[96:99], v[48:63]\n"
"ds_read_b64_tr_b16 v[88:89], v180 offset:47104\n"
"ds_read_b64_tr_b16 v[90:91], v180 offset:47616\n"
"v_mfma_f32_16x16x32_bf16 v[68:71], v[192:195], v[92:95], v[68:71]\n"
"buffer_load_dwordx4 v10, s[24:27], s65 offen offset:1024 lds\n"
"s_add_u32 s4, s4, 1\n"
"s_and_b32 s4, s4, 7\n"
"v_mfma_f32_16x16x32_bf16 v[72:75], v[192:195], v[96:99], v[72:75]\n"
"s_waitcnt vmcnt(7)\n"
"s_waitcnt lgkmcnt(0)\n"
"s_barrier\n"
"v_mfma_f32_32x32x16_bf16 v[32:47], a[152:155], v[76:79], v[32:47]\n"
"ds_read_b128 a[160:163], v181 offset:0\n"
"ds_read_b64_tr_b16 v[92:93], v185 offset:8192\n"
"ds_read_b64_tr_b16 v[94:95], v185 offset:8704\n"
"v_mfma_f32_32x32x16_bf16 v[48:63], a[152:155], v[80:83], v[48:63]\n"
"ds_read_b64_tr_b16 v[96:97], v185 offset:16384\n"
"ds_read_b64_tr_b16 v[98:99], v185 offset:16896\n"
"s_add_u32 m0, s75, 24576\n"
"s_lshl_b32 s63, s4, 7\n"
"s_add_u32 s63, s63, s5\n"
"buffer_load_dwordx4 v7, s[20:23], s63 offen lds\n"
"v_mfma_f32_16x16x32_bf16 v[68:71], v[192:195], v[76:79], v[68:71]\n"
"v_mfma_f32_16x16x32_bf16 v[72:75], v[192:195], v[80:83], v[72:75]\n"
"buffer_load_dwordx4 v8, s[20:23], s63 offen offset:1024 lds\n"
"s_mul_i32 s7, s4, 196608\n"
"s_add_u32 s7, s7, s6\n"
"v_mfma_f32_32x32x16_bf16 v[32:47], a[156:159], v[84:87], v[32:47]\n"
"ds_read_b128 a[164:167], v182 offset:0\n"
"ds_read_b64_tr_b16 v[76:77], v185 offset:10240\n"
"ds_read_b64_tr_b16 v[78:79], v185 offset:10752\n"
"v_mfma_f32_32x32x16_bf16 v[48:63], a[156:159], v[88:91], v[48:63]\n"
"ds_read_b64_tr_b16 v[80:81], v185 offset:18432\n"
"ds_read_b64_tr_b16 v[82:83], v185 offset:18944\n"
"v_mfma_f32_16x16x32_bf16 v[68:71], v[192:195], v[84:87], v[68:71]\n"
"s_add_u32 m0, s75, 32768\n"
"s_add_u32 s64, s7, 1024\n"
"buffer_load_dwordx4 v9, s[24:27], s64 offen lds\n"
"v_mfma_f32_16x16x32_bf16 v[72:75], v[192:195], v[88:91], v[72:75]\n"
"s_waitcnt lgkmcnt(7)\n"
"v_mfma_f32_32x32x16_bf16 v[32:47], a[160:163], v[92:95], v[32:47]\n"
"ds_read_b128 a[168:171], v183 offset:0\n"
"ds_read_b64_tr_b16 v[84:85], v185 offset:12288\n"
"ds_read_b64_tr_b16 v[86:87], v185 offset:12800\n"
"s_waitcnt lgkmcnt(8)\n"
"v_mfma_f32_32x32x16_bf16 v[48:63], a[160:163], v[96:99], v[48:63]\n"
"ds_read_b64_tr_b16 v[88:89], v185 offset:20480\n"
"ds_read_b64_tr_b16 v[90:91], v185 offset:20992\n"
"buffer_load_dwordx4 v10, s[24:27], s64 offen offset:1024 lds\n"
"v_mfma_f32_16x16x32_bf16 v[68:71], v[192:195], v[92:95], v[68:71]\n"
"v_mfma_f32_16x16x32_bf16 v[72:75], v[192:195], v[96:99], v[72:75]\n"
"s_add_u32 m0, s75, 40960\n"
"s_add_u32 s65, s7, 2048\n"
"buffer_load_dwordx4 v9, s[24:27], s65 offen lds\n"
"s_waitcnt lgkmcnt(7)\n"
"v_mfma_f32_32x32x16_bf16 v[32:47], a[164:167], v[76:79], v[32:47]\n"
"ds_read_b128 a[172:175], v184 offset:0\n"
"ds_read_b64_tr_b16 v[92:93], v185 offset:14336\n"
"ds_read_b64_tr_b16 v[94:95], v185 offset:14848\n"
"s_waitcnt lgkmcnt(8)\n"
"v_mfma_f32_32x32x16_bf16 v[48:63], a[164:167], v[80:83], v[48:63]\n"
"ds_read_b64_tr_b16 v[96:97], v185 offset:22528\n"
"ds_read_b64_tr_b16 v[98:99], v185 offset:23040\n"
"v_mfma_f32_16x16x32_bf16 v[68:71], v[192:195], v[76:79], v[68:71]\n"
"buffer_load_dwordx4 v10, s[24:27], s65 offen offset:1024 lds\n"
"s_add_u32 s4, s4, 1\n"
"s_and_b32 s4, s4, 7\n"
"v_mfma_f32_16x16x32_bf16 v[72:75], v[192:195], v[80:83], v[72:75]\n"
"s_waitcnt vmcnt(6)\n"
"s_waitcnt lgkmcnt(0)\n"
"s_barrier\n"
"v_mfma_f32_32x32x16_bf16 v[32:47], a[168:171], v[84:87], v[32:47]\n"
"ds_read_b128 a[176:179], v176 offset:0\n"
"ds_read_b64_tr_b16 v[76:77], v180 offset:8192\n"
"ds_read_b64_tr_b16 v[78:79], v180 offset:8704\n"
"v_mfma_f32_32x32x16_bf16 v[48:63], a[168:171], v[88:91], v[48:63]\n"
"ds_read_b64_tr_b16 v[80:81], v180 offset:16384\n"
"ds_read_b64_tr_b16 v[82:83], v180 offset:16896\n"
"s_add_u32 m0, s75, 49152\n"
"s_lshl_b32 s63, s4, 7\n"
"s_add_u32 s63, s63, s5\n"
"buffer_load_dwordx4 v7, s[20:23], s63 offen lds\n"
"v_mfma_f32_16x16x32_bf16 v[68:71], v[192:195], v[84:87], v[68:71]\n"
"v_mfma_f32_16x16x32_bf16 v[72:75], v[192:195], v[88:91], v[72:75]\n"
"buffer_load_dwordx4 v8, s[20:23], s63 offen offset:1024 lds\n"
"s_mul_i32 s7, s4, 196608\n"
"s_add_u32 s7, s7, s6\n"
"v_mfma_f32_32x32x16_bf16 v[32:47], a[172:175], v[92:95], v[32:47]\n"
"ds_read_b128 a[180:183], v177 offset:0\n"
"ds_read_b64_tr_b16 v[84:85], v180 offset:10240\n"
"ds_read_b64_tr_b16 v[86:87], v180 offset:10752\n"
"v_mfma_f32_32x32x16_bf16 v[48:63], a[172:175], v[96:99], v[48:63]\n"
"ds_read_b64_tr_b16 v[88:89], v180 offset:18432\n"
"ds_read_b64_tr_b16 v[90:91], v180 offset:18944\n"
"v_mfma_f32_16x16x32_bf16 v[68:71], v[192:195], v[92:95], v[68:71]\n"
"s_add_u32 m0, s75, 57344\n"
"s_add_u32 s64, s7, 1024\n"
"buffer_load_dwordx4 v9, s[24:27], s64 offen lds\n"
"v_mfma_f32_16x16x32_bf16 v[72:75], v[192:195], v[96:99], v[72:75]\n"
"s_waitcnt lgkmcnt(7)\n"
"v_mfma_f32_32x32x16_bf16 v[32:47], a[176:179], v[76:79], v[32:47]\n"
"ds_read_b128 a[184:187], v178 offset:0\n"
"ds_read_b64_tr_b16 v[92:93], v180 offset:12288\n"
"ds_read_b64_tr_b16 v[94:95], v180 offset:12800\n"
"s_waitcnt lgkmcnt(8)\n"
"v_mfma_f32_32x32x16_bf16 v[48:63], a[176:179], v[80:83], v[48:63]\n"
"ds_read_b64_tr_b16 v[96:97], v180 offset:20480\n"
"ds_read_b64_tr_b16 v[98:99], v180 offset:20992\n"
"buffer_load_dwordx4 v10, s[24:27], s64 offen offset:1024 lds\n"
"v_mfma_f32_16x16x32_bf16 v[68:71], v[192:195], v[76:79], v[68:71]\n"
"v_mfma_f32_16x16x32_bf16 v[72:75], v[192:195], v[80:83], v[72:75]\n"
"s_add_u32 m0, s75, 65536\n"
"s_add_u32 s65, s7, 2048\n"
"buffer_load_dwordx4 v9, s[24:27], s65 offen lds\n"
"s_waitcnt lgkmcnt(7)\n"
"v_mfma_f32_32x32x16_bf16 v[32:47], a[180:183], v[84:87], v[32:47]\n"
"ds_read_b128 a[188:191], v179 offset:0\n"
"ds_read_b64_tr_b16 v[76:77], v180 offset:14336\n"
"ds_read_b64_tr_b16 v[78:79], v180 offset:14848\n"
"s_waitcnt lgkmcnt(8)\n"
"v_mfma_f32_32x32x16_bf16 v[48:63], a[180:183], v[88:91], v[48:63]\n"
"ds_read_b64_tr_b16 v[80:81], v180 offset:22528\n"
"ds_read_b64_tr_b16 v[82:83], v180 offset:23040\n"
"v_mfma_f32_16x16x32_bf16 v[68:71], v[192:195], v[84:87], v[68:71]\n"
"buffer_load_dwordx4 v10, s[24:27], s65 offen offset:1024 lds\n"
"s_add_u32 s4, s4, 1\n"
"s_and_b32 s4, s4, 7\n"
"v_mfma_f32_16x16x32_bf16 v[72:75], v[192:195], v[88:91], v[72:75]\n"
"s_waitcnt vmcnt(6)\n"
"s_waitcnt lgkmcnt(0)\n"
"s_barrier\n"
"v_mfma_f32_32x32x16_bf16 v[32:47], a[184:187], v[92:95], v[32:47]\n"
"ds_read_b128 a[192:195], v176 offset:24576\n"
"ds_read_b64_tr_b16 v[84:85], v180 offset:32768\n"
"ds_read_b64_tr_b16 v[86:87], v180 offset:33280\n"
"v_mfma_f32_32x32x16_bf16 v[48:63], a[184:187], v[96:99], v[48:63]\n"
"ds_read_b64_tr_b16 v[88:89], v180 offset:40960\n"
"ds_read_b64_tr_b16 v[90:91], v180 offset:41472\n"
"s_add_u32 m0, s75, 0\n"
"s_lshl_b32 s63, s4, 7\n"
"s_add_u32 s63, s63, s5\n"
"buffer_load_dwordx4 v7, s[20:23], s63 offen lds\n"
"v_mfma_f32_16x16x32_bf16 v[68:71], v[192:195], v[92:95], v[68:71]\n"
"v_mfma_f32_16x16x32_bf16 v[72:75], v[192:195], v[96:99], v[72:75]\n"
"buffer_load_dwordx4 v8, s[20:23], s63 offen offset:1024 lds\n"
"s_mul_i32 s7, s4, 196608\n"
"s_add_u32 s7, s7, s6\n"
"v_mfma_f32_32x32x16_bf16 v[32:47], a[188:191], v[76:79], v[32:47]\n"
"ds_read_b128 a[196:199], v177 offset:24576\n"
"ds_read_b64_tr_b16 v[92:93], v180 offset:34816\n"
"ds_read_b64_tr_b16 v[94:95], v180 offset:35328\n"
"v_mfma_f32_32x32x16_bf16 v[48:63], a[188:191], v[80:83], v[48:63]\n"
"ds_read_b64_tr_b16 v[96:97], v180 offset:43008\n"
"ds_read_b64_tr_b16 v[98:99], v180 offset:43520\n"
"v_mfma_f32_16x16x32_bf16 v[68:71], v[192:195], v[76:79], v[68:71]\n"
"s_add_u32 m0, s75, 8192\n"
"s_add_u32 s64, s7, 1024\n"
"buffer_load_dwordx4 v9, s[24:27], s64 offen lds\n"
"v_mfma_f32_16x16x32_bf16 v[72:75], v[192:195], v[80:83], v[72:75]\n"
"s_waitcnt lgkmcnt(7)\n"
"v_mfma_f32_32x32x16_bf16 v[32:47], a[192:195], v[84:87], v[32:47]\n"
"ds_read_b128 a[200:203], v178 offset:24576\n"
"ds_read_b64_tr_b16 v[76:77], v180 offset:36864\n"
"ds_read_b64_tr_b16 v[78:79], v180 offset:37376\n"
"s_waitcnt lgkmcnt(8)\n"
"v_mfma_f32_32x32x16_bf16 v[48:63], a[192:195], v[88:91], v[48:63]\n"
"ds_read_b64_tr_b16 v[80:81], v180 offset:45056\n"
"ds_read_b64_tr_b16 v[82:83], v180 offset:45568\n"
"buffer_load_dwordx4 v10, s[24:27], s64 offen offset:1024 lds\n"
"v_mfma_f32_16x16x32_bf16 v[68:71], v[192:195], v[84:87], v[68:71]\n"
"v_mfma_f32_16x16x32_bf16 v[72:75], v[192:195], v[88:91], v[72:75]\n"
"s_add_u32 m0, s75, 16384\n"
"s_add_u32 s65, s7, 2048\n"
"buffer_load_dwordx4 v9, s[24:27], s65 offen lds\n"
"s_waitcnt lgkmcnt(7)\n"
"v_mfma_f32_32x32x16_bf16 v[32:47], a[196:199], v[92:95], v[32:47]\n"
"ds_read_b128 a[204:207], v179 offset:24576\n"
"ds_read_b64_tr_b16 v[84:85], v180 offset:38912\n"
"ds_read_b64_tr_b16 v[86:87], v180 offset:39424\n"
"s_waitcnt lgkmcnt(8)\n"
"v_mfma_f32_32x32x16_bf16 v[48:63], a[196:199], v[96:99], v[48:63]\n"
"ds_read_b64_tr_b16 v[88:89], v180 offset:47104\n"
"ds_read_b64_tr_b16 v[90:91], v180 offset:47616\n"
"v_mfma_f32_16x16x32_bf16 v[68:71], v[192:195], v[92:95], v[68:71]\n"
"buffer_load_dwordx4 v10, s[24:27], s65 offen offset:1024 lds\n"
"s_add_u32 s4, s4, 1\n"
"s_and_b32 s4, s4, 7\n"
"v_mfma_f32_16x16x32_bf16 v[72:75], v[192:195], v[96:99], v[72:75]\n"
"s_waitcnt vmcnt(6)\n"
"s_waitcnt lgkmcnt(0)\n"
"s_barrier\n"
"v_mfma_f32_32x32x16_bf16 v[32:47], a[200:203], v[76:79], v[32:47]\n"
"ds_read_b128 a[208:211], v181 offset:0\n"
"ds_read_b64_tr_b16 v[92:93], v185 offset:8192\n"
"ds_read_b64_tr_b16 v[94:95], v185 offset:8704\n"
"v_mfma_f32_32x32x16_bf16 v[48:63], a[200:203], v[80:83], v[48:63]\n"
"ds_read_b64_tr_b16 v[96:97], v185 offset:16384\n"
"ds_read_b64_tr_b16 v[98:99], v185 offset:16896\n"
"s_lshl_b32 s10, s47, 10\n"
"s_lshr_b32 s66, s45, 1\n"
"s_lshl_b32 s66, s66, 9\n"
"s_add_u32 s10, s10, s66\n"
"s_add_u32 s10, s10, 4096\n"
"v_lshlrev_b32 v15, 4, v186\n"
"buffer_load_dwordx4 v[140:143], v15, s[16:19], s10 offen sc1\n"
"v_mfma_f32_16x16x32_bf16 v[68:71], v[192:195], v[76:79], v[68:71]\n"
"s_add_u32 m0, s75, 24576\n"
"s_lshl_b32 s63, s4, 7\n"
"s_add_u32 s63, s63, s5\n"
"buffer_load_dwordx4 v7, s[20:23], s63 offen lds\n"
"v_mfma_f32_16x16x32_bf16 v[72:75], v[192:195], v[80:83], v[72:75]\n"
"v_mfma_f32_32x32x16_bf16 v[32:47], a[204:207], v[84:87], v[32:47]\n"
"ds_read_b128 a[212:215], v182 offset:0\n"
"ds_read_b64_tr_b16 v[76:77], v185 offset:10240\n"
"ds_read_b64_tr_b16 v[78:79], v185 offset:10752\n"
"v_mfma_f32_32x32x16_bf16 v[48:63], a[204:207], v[88:91], v[48:63]\n"
"ds_read_b64_tr_b16 v[80:81], v185 offset:18432\n"
"ds_read_b64_tr_b16 v[82:83], v185 offset:18944\n"
"buffer_load_dwordx4 v8, s[20:23], s63 offen offset:1024 lds\n"
"s_mul_i32 s7, s4, 196608\n"
"s_add_u32 s7, s7, s6\n"
"v_mfma_f32_16x16x32_bf16 v[68:71], v[192:195], v[84:87], v[68:71]\n"
"v_mfma_f32_16x16x32_bf16 v[72:75], v[192:195], v[88:91], v[72:75]\n"
"s_add_u32 m0, s75, 32768\n"
"s_add_u32 s64, s7, 1024\n"
"buffer_load_dwordx4 v9, s[24:27], s64 offen lds\n"
"s_waitcnt lgkmcnt(7)\n"
"v_mfma_f32_32x32x16_bf16 v[32:47], a[208:211], v[92:95], v[32:47]\n"
"ds_read_b128 a[216:219], v183 offset:0\n"
"ds_read_b64_tr_b16 v[84:85], v185 offset:12288\n"
"ds_read_b64_tr_b16 v[86:87], v185 offset:12800\n"
"s_waitcnt lgkmcnt(8)\n"
"v_mfma_f32_32x32x16_bf16 v[48:63], a[208:211], v[96:99], v[48:63]\n"
"ds_read_b64_tr_b16 v[88:89], v185 offset:20480\n"
"ds_read_b64_tr_b16 v[90:91], v185 offset:20992\n"
"buffer_load_dwordx4 v10, s[24:27], s64 offen offset:1024 lds\n"
"v_mfma_f32_16x16x32_bf16 v[68:71], v[192:195], v[92:95], v[68:71]\n"
"v_mfma_f32_16x16x32_bf16 v[72:75], v[192:195], v[96:99], v[72:75]\n"
"s_add_u32 m0, s75, 40960\n"
"s_add_u32 s65, s7, 2048\n"
"buffer_load_dwordx4 v9, s[24:27], s65 offen lds\n"
"s_waitcnt lgkmcnt(7)\n"
"v_mfma_f32_32x32x16_bf16 v[32:47], a[212:215], v[76:79], v[32:47]\n"
"ds_read_b128 a[220:223], v184 offset:0\n"
"ds_read_b64_tr_b16 v[92:93], v185 offset:14336\n"
"ds_read_b64_tr_b16 v[94:95], v185 offset:14848\n"
"s_waitcnt lgkmcnt(8)\n"
"v_mfma_f32_32x32x16_bf16 v[48:63], a[212:215], v[80:83], v[48:63]\n"
"ds_read_b64_tr_b16 v[96:97], v185 offset:22528\n"
"ds_read_b64_tr_b16 v[98:99], v185 offset:23040\n"
"v_mfma_f32_16x16x32_bf16 v[68:71], v[192:195], v[76:79], v[68:71]\n"
"buffer_load_dwordx4 v10, s[24:27], s65 offen offset:1024 lds\n"
"s_add_u32 s4, s4, 1\n"
"s_and_b32 s4, s4, 7\n"
"v_mfma_f32_16x16x32_bf16 v[72:75], v[192:195], v[80:83], v[72:75]\n"
"s_waitcnt vmcnt(7)\n"
"s_waitcnt lgkmcnt(0)\n"
"s_barrier\n"
"v_mfma_f32_32x32x16_bf16 v[32:47], a[216:219], v[84:87], v[32:47]\n"
"ds_read_b128 a[224:227], v176 offset:0\n"
"ds_read_b64_tr_b16 v[76:77], v180 offset:8192\n"
"ds_read_b64_tr_b16 v[78:79], v180 offset:8704\n"
"v_mfma_f32_32x32x16_bf16 v[48:63], a[216:219], v[88:91], v[48:63]\n"
"ds_read_b64_tr_b16 v[80:81], v180 offset:16384\n"
"ds_read_b64_tr_b16 v[82:83], v180 offset:16896\n"
"s_mov_b32 m0, s77\n"
"s_nop 0\n"
"buffer_load_dwordx4 v196, s[32:35], s8 offen lds\n"
"buffer_load_dwordx4 v197, s[32:35], s8 offen offset:1024 lds\n"
"v_mfma_f32_16x16x32_bf16 v[68:71], v[192:195], v[84:87], v[68:71]\n"
"v_mfma_f32_16x16x32_bf16 v[72:75], v[192:195], v[88:91], v[72:75]\n"
"s_add_u32 m0, s77, 2048\n"
"s_nop 0\n"
"buffer_load_dwordx4 v196, s[36:39], s8 offen lds\n"
"buffer_load_dwordx4 v197, s[36:39], s8 offen offset:1024 lds\n"
"v_mfma_f32_32x32x16_bf16 v[32:47], a[220:223], v[92:95], v[32:47]\n"
"ds_read_b128 a[228:231], v177 offset:0\n"
"ds_read_b64_tr_b16 v[84:85], v180 offset:10240\n"
"ds_read_b64_tr_b16 v[86:87], v180 offset:10752\n"
"v_mfma_f32_32x32x16_bf16 v[48:63], a[220:223], v[96:99], v[48:63]\n"
"ds_read_b64_tr_b16 v[88:89], v180 offset:18432\n"
"ds_read_b64_tr_b16 v[90:91], v180 offset:18944\n"
"v_mfma_f32_16x16x32_bf16 v[68:71], v[192:195], v[92:95], v[68:71]\n"
"s_add_u32 m0, s75, 73728\n"
"s_mul_i32 s7, s4, 196608\n"
"s_add_u32 s7, s7, s6\n"
"buffer_load_dwordx4 v250, s[24:27], s7 offen lds\n"
"v_mfma_f32_16x16x32_bf16 v[72:75], v[192:195], v[96:99], v[72:75]\n"
"s_waitcnt lgkmcnt(7)\n"
"v_mfma_f32_32x32x16_bf16 v[32:47], a[224:227], v[76:79], v[32:47]\n"
"ds_read_b128 a[232:235], v178 offset:0\n"
"ds_read_b64_tr_b16 v[92:93], v180 offset:12288\n"
"ds_read_b64_tr_b16 v[94:95], v180 offset:12800\n"
"s_waitcnt lgkmcnt(8)\n"
"v_mfma_f32_32x32x16_bf16 v[48:63], a[224:227], v[80:83], v[48:63]\n"
"ds_read_b64_tr_b16 v[96:97], v180 offset:20480\n"
"ds_read_b64_tr_b16 v[98:99], v180 offset:20992\n"
"buffer_load_dwordx4 v10, s[24:27], s7 offen offset:1024 lds\n"
"s_add_u32 s4, s4, 1\n"
"s_and_b32 s4, s4, 7\n"
"v_mfma_f32_16x16x32_bf16 v[68:71], v[192:195], v[76:79], v[68:71]\n"
"v_mfma_f32_16x16x32_bf16 v[72:75], v[192:195], v[80:83], v[72:75]\n"
"s_add_u32 m0, s75, 81920\n"
"s_mul_i32 s7, s4, 196608\n"
"s_add_u32 s7, s7, s6\n"
"buffer_load_dwordx4 v250, s[24:27], s7 offen lds\n"
"s_waitcnt lgkmcnt(7)\n"
"v_mfma_f32_32x32x16_bf16 v[32:47], a[228:231], v[84:87], v[32:47]\n"
"ds_read_b128 a[236:239], v179 offset:0\n"
"ds_read_b64_tr_b16 v[76:77], v180 offset:14336\n"
"ds_read_b64_tr_b16 v[78:79], v180 offset:14848\n"
"s_waitcnt lgkmcnt(8)\n"
"v_mfma_f32_32x32x16_bf16 v[48:63], a[228:231], v[88:91], v[48:63]\n"
"ds_read_b64_tr_b16 v[80:81], v180 offset:22528\n"
"ds_read_b64_tr_b16 v[82:83], v180 offset:23040\n"
"v_mfma_f32_16x16x32_bf16 v[68:71], v[192:195], v[84:87], v[68:71]\n"
"buffer_load_dwordx4 v10, s[24:27], s7 offen offset:1024 lds\n"
"s_add_u32 s4, s4, 1\n"
"s_and_b32 s4, s4, 7\n"
"v_mfma_f32_16x16x32_bf16 v[72:75], v[192:195], v[88:91], v[72:75]\n"
"s_waitcnt vmcnt(8)\n"
"s_waitcnt lgkmcnt(0)\n"
"s_barrier\n"
"v_cmp_ne_u32 vcc, s44, v141\n"
"v_cmp_ne_u32 s[70:71], s44, v143\n"
"s_or_b64 vcc, vcc, s[70:71]\n"
"s_cbranch_vccz .LK_abok_1\n"
"s_mov_b32 s53, 32768\n"
".LK_absp_2:\n"
"buffer_load_dwordx4 v[140:143], v15, s[16:19], s10 offen sc1\n"
"s_waitcnt vmcnt(0)\n"
"v_cmp_ne_u32 vcc, s44, v141\n"
"v_cmp_ne_u32 s[70:71], s44, v143\n"
"s_or_b64 vcc, vcc, s[70:71]\n"
"s_cbranch_vccz .LK_abok_1\n"
"s_sub_u32 s53, s53, 1\n"
"s_cbranch_scc0 .LK_absp_2\n"
".LK_abok_1:\n"
"v_mfma_f32_32x32x16_bf16 v[32:47], a[232:235], v[92:95], v[32:47]\n"
"ds_read_b128 a[240:243], v176 offset:24576\n"
"ds_read_b64_tr_b16 v[84:85], v180 offset:32768\n"
"ds_read_b64_tr_b16 v[86:87], v180 offset:33280\n"
"v_mfma_f32_32x32x16_bf16 v[48:63], a[232:235], v[96:99], v[48:63]\n"
"ds_read_b64_tr_b16 v[88:89], v180 offset:40960\n"
"ds_read_b64_tr_b16 v[90:91], v180 offset:41472\n"
"s_add_u32 m0, s75, 90112\n"
"s_mul_i32 s7, s4, 196608\n"
"s_add_u32 s7, s7, s6\n"
"buffer_load_dwordx4 v250, s[24:27], s7 offen lds\n"
"v_mfma_f32_16x16x32_bf16 v[68:71], v[192:195], v[92:95], v[68:71]\n"
"s_lshl_b32 s69, s45, 9\n"
"s_add_u32 s69, s69, 65536\n"
"v_mfma_f32_16x16x32_bf16 v[72:75], v[192:195], v[96:99], v[72:75]\n"
"v_lshlrev_b32 v12, 2, v186\n"
"v_add_u32 v249, s69, v12\n"
"v_mfma_f32_32x32x16_bf16 v[32:47], a[236:239], v[76:79], v[32:47]\n"
"ds_read_b128 a[244:247], v177 offset:24576\n"
"ds_read_b64_tr_b16 v[92:93], v180 offset:34816\n"
"ds_read_b64_tr_b16 v[94:95], v180 offset:35328\n"
"ds_write_b32 v249, v140\n"
"ds_write_b32 v249, v142 offset:128\n"
"v_mfma_f32_32x32x16_bf16 v[48:63], a[236:239], v[80:83], v[48:63]\n"
"ds_read_b64_tr_b16 v[96:97], v180 offset:43008\n"
"ds_read_b64_tr_b16 v[98:99], v180 offset:43520\n"
"buffer_load_dwordx4 v10, s[24:27], s7 offen offset:1024 lds\n"
"s_add_u32 s4, s4, 1\n"
"s_and_b32 s4, s4, 7\n"
"v_lshlrev_b32 v12, 4, v5\n"
"v_mfma_f32_16x16x32_bf16 v[68:71], v[192:195], v[76:79], v[68:71]\n"
"v_add_u32 v12, s69, v12\n"
"ds_read_b128 v[108:111], v12 offset:0\n"
"v_mfma_f32_16x16x32_bf16 v[72:75], v[192:195], v[80:83], v[72:75]\n"
"s_add_u32 m0, s75, 98304\n"
"s_mul_i32 s7, s4, 196608\n"
"s_add_u32 s7, s7, s6\n"
"buffer_load_dwordx4 v250, s[24:27], s7 offen lds\n"
"s_waitcnt lgkmcnt(10)\n"
"v_mfma_f32_32x32x16_bf16 v[32:47], a[240:243], v[84:87], v[32:47]\n"
"ds_read_b128 a[248:251], v178 offset:24576\n"
"ds_read_b64_tr_b16 v[76:77], v180 offset:36864\n"
"ds_read_b64_tr_b16 v[78:79], v180 offset:37376\n"
"ds_read_b128 v[112:115], v12 offset:32\n"
"s_waitcnt lgkmcnt(12)\n"
"v_mfma_f32_32x32x16_bf16 v[48:63], a[240:243], v[88:91], v[48:63]\n"
"ds_read_b64_tr_b16 v[80:81], v180 offset:45056\n"
"ds_read_b64_tr_b16 v[82:83], v180 offset:45568\n"
"ds_read_b128 v[116:119], v12 offset:64\n"
"ds_read_b128 v[120:123], v12 offset:96\n"
"ds_read_b128 v[124:127], v12 offset:128\n"
"v_mfma_f32_16x16x32_bf16 v[68:71], v[192:195], v[84:87], v[68:71]\n"
"buffer_load_dwordx4 v10, s[24:27], s7 offen offset:1024 lds\n"
"s_add_u32 s4, s4, 1\n"
"s_and_b32 s4, s4, 7\n"
"v_mfma_f32_16x16x32_bf16 v[72:75], v[192:195], v[88:91], v[72:75]\n"
"ds_read_b128 v[128:131], v12 offset:160\n"
"s_waitcnt lgkmcnt(15)\n"
"v_mfma_f32_32x32x16_bf16 v[32:47], a[244:247], v[92:95], v[32:47]\n"
"ds_read_b128 a[252:255], v179 offset:24576\n"
"ds_read_b64_tr_b16 v[84:85], v180 offset:38912\n"
"ds_read_b64_tr_b16 v[86:87], v180 offset:39424\n"
"ds_read_b128 v[132:135], v12 offset:192\n"
"ds_read_b128 v[136:139], v12 offset:224\n"
"s_waitcnt lgkmcnt(15)\n"
"v_mfma_f32_32x32x16_bf16 v[48:63], a[244:247], v[96:99], v[48:63]\n"
"ds_read_b64_tr_b16 v[88:89], v180 offset:47104\n"
"ds_read_b64_tr_b16 v[90:91], v180 offset:47616\n"
"s_add_u32 m0, s75, 106496\n"
"s_mul_i32 s7, s4, 196608\n"
"s_add_u32 s7, s7, s6\n"
"buffer_load_dwordx4 v250, s[24:27], s7 offen lds\n"
"v_mfma_f32_16x16x32_bf16 v[68:71], v[192:195], v[92:95], v[68:71]\n"
"v_mfma_f32_16x16x32_bf16 v[72:75], v[192:195], v[96:99], v[72:75]\n"
"buffer_load_dwordx4 v10, s[24:27], s7 offen offset:1024 lds\n"
"s_add_u32 s4, s4, 1\n"
"s_and_b32 s4, s4, 7\n"
"s_waitcnt lgkmcnt(14)\n"
"v_mfma_f32_32x32x16_bf16 v[32:47], a[248:251], v[76:79], v[32:47]\n"
"s_waitcnt lgkmcnt(11)\n"
"v_mfma_f32_32x32x16_bf16 v[48:63], a[248:251], v[80:83], v[48:63]\n"
"v_mfma_f32_16x16x32_bf16 v[68:71], v[192:195], v[76:79], v[68:71]\n"
"s_add_u32 m0, s75, 114688\n"
"s_mul_i32 s7, s4, 196608\n"
"s_add_u32 s7, s7, s6\n"
"buffer_load_dwordx4 v250, s[24:27], s7 offen lds\n"
"v_mfma_f32_16x16x32_bf16 v[72:75], v[192:195], v[80:83], v[72:75]\n"
"s_waitcnt lgkmcnt(4)\n"
"v_mfma_f32_32x32x16_bf16 v[32:47], a[252:255], v[84:87], v[32:47]\n"
"s_waitcnt lgkmcnt(0)\n"
"v_mfma_f32_32x32x16_bf16 v[48:63], a[252:255], v[88:91], v[48:63]\n"
"buffer_load_dwordx4 v10, s[24:27], s7 offen offset:1024 lds\n"
"s_add_u32 s4, s4, 1\n"
"s_and_b32 s4, s4, 7\n"
"v_mfma_f32_16x16x32_bf16 v[68:71], v[192:195], v[84:87], v[68:71]\n"
"v_mfma_f32_16x16x32_bf16 v[72:75], v[192:195], v[88:91], v[72:75]\n"
"s_lshl_b32 s67, s45, 12\n"
"s_add_u32 s67, s67, 49152\n"
"v_add_u32 v15, s67, v6\n"
"s_and_b32 s68, s45, 1\n"
"s_lshl_b32 s68, s68, 8\n"
"s_add_u32 s69, s75, 49152\n"
"v_bfe_u32 v13, v1, 1, 1\n"
"v_lshlrev_b32 v13, 10, v13\n"
"v_bfe_u32 v14, v1, 2, 1\n"
"v_lshl_add_u32 v13, v14, 9, v13\n"
"v_and_b32 v14, 1, v1\n"
"v_lshl_add_u32 v13, v14, 3, v13\n"
"v_add_u32 v13, s69, v13\n"
"s_sub_u32 s70, 256, s68\n"
"v_add_u32 v14, s70, v13\n"
"v_lshl_add_u32 v13, v2, 4, v13\n"
"v_add_u32 v13, s68, v13\n"
"v_mov_b32 v12, s62\n"
"v_pk_fma_f32 v[76:77], v[124:125], v[68:69], s[54:55]\n"
"v_pk_fma_f32 v[80:81], v[126:127], v[68:69], s[54:55]\n"
"v_pk_fma_f32 v[84:85], v[128:129], v[68:69], s[54:55]\n"
"v_pk_fma_f32 v[88:89], v[130:131], v[68:69], s[54:55]\n"
"v_pk_fma_f32 v[76:77], v[108:109], v[32:33], v[76:77]\n"
"v_pk_fma_f32 v[80:81], v[110:111], v[34:35], v[80:81]\n"
"v_pk_fma_f32 v[84:85], v[112:113], v[36:37], v[84:85]\n"
"v_pk_fma_f32 v[88:89], v[114:115], v[38:39], v[88:89]\n"
"v_pk_mul_f32 v[78:79], v[76:77], s[56:57]\n"
"v_pk_mul_f32 v[82:83], v[80:81], s[56:57]\n"
"v_pk_mul_f32 v[86:87], v[84:85], s[56:57]\n"
"v_pk_mul_f32 v[90:91], v[88:89], s[56:57]\n"
"v_exp_f32_e64 v78, v78 clamp\n"
"v_max_f32 v76, s54, v76\n"
"v_exp_f32_e64 v79, v79 clamp\n"
"v_max_f32 v77, s54, v77\n"
"v_exp_f32_e64 v82, v82 clamp\n"
"v_max_f32 v80, s54, v80\n"
"v_exp_f32_e64 v83, v83 clamp\n"
"v_max_f32 v81, s54, v81\n"
"v_exp_f32_e64 v86, v86 clamp\n"
"v_max_f32 v84, s54, v84\n"
"v_exp_f32_e64 v87, v87 clamp\n"
"v_max_f32 v85, s54, v85\n"
"v_exp_f32_e64 v90, v90 clamp\n"
"v_max_f32 v88, s54, v88\n"
"v_exp_f32_e64 v91, v91 clamp\n"
"v_max_f32 v89, s54, v89\n"
"v_pk_add_f32 v[76:77], v[76:77], v[78:79]\n"
"v_pk_add_f32 v[80:81], v[80:81], v[82:83]\n"
"v_pk_add_f32 v[84:85], v[84:85], v[86:87]\n"
"v_pk_add_f32 v[88:89], v[88:89], v[90:91]\n"
"v_cvt_pk_bf16_f32 v144, v76, v77\n"
"v_cvt_pk_bf16_f32 v145, v80, v81\n"
"v_cvt_pk_bf16_f32 v146, v84, v85\n"
"v_cvt_pk_bf16_f32 v147, v88, v89\n"
"v_pk_fma_f32 v[76:77], v[132:133], v[68:69], s[54:55]\n"
"v_pk_fma_f32 v[80:81], v[134:135], v[68:69], s[54:55]\n"
"v_pk_fma_f32 v[84:85], v[136:137], v[68:69], s[54:55]\n"
"v_pk_fma_f32 v[88:89], v[138:139], v[68:69], s[54:55]\n"
"v_pk_fma_f32 v[76:77], v[116:117], v[40:41], v[76:77]\n"
"v_pk_fma_f32 v[80:81], v[118:119], v[42:43], v[80:81]\n"
"v_pk_fma_f32 v[84:85], v[120:121], v[44:45], v[84:85]\n"
"v_pk_fma_f32 v[88:89], v[122:123], v[46:47], v[88:89]\n"
"v_pk_mul_f32 v[78:79], v[76:77], s[56:57]\n"
"v_pk_mul_f32 v[82:83], v[80:81], s[56:57]\n"
"v_pk_mul_f32 v[86:87], v[84:85], s[56:57]\n"
"v_pk_mul_f32 v[90:91], v[88:89], s[56:57]\n"
"v_exp_f32_e64 v78, v78 clamp\n"
"v_max_f32 v76, s54, v76\n"
"v_exp_f32_e64 v79, v79 clamp\n"
"v_max_f32 v77, s54, v77\n"
"v_exp_f32_e64 v82, v82 clamp\n"
"v_max_f32 v80, s54, v80\n"
"v_exp_f32_e64 v83, v83 clamp\n"
"v_max_f32 v81, s54, v81\n"
"v_exp_f32_e64 v86, v86 clamp\n"
"v_max_f32 v84, s54, v84\n"
"v_exp_f32_e64 v87, v87 clamp\n"
"v_max_f32 v85, s54, v85\n"
"v_exp_f32_e64 v90, v90 clamp\n"
"v_max_f32 v88, s54, v88\n"
"v_exp_f32_e64 v91, v91 clamp\n"
"v_max_f32 v89, s54, v89\n"
"v_pk_add_f32 v[76:77], v[76:77], v[78:79]\n"
"v_pk_add_f32 v[80:81], v[80:81], v[82:83]\n"
"v_pk_add_f32 v[84:85], v[84:85], v[86:87]\n"
"v_pk_add_f32 v[88:89], v[88:89], v[90:91]\n"
"v_cvt_pk_bf16_f32 v148, v76, v77\n"
"v_cvt_pk_bf16_f32 v149, v80, v81\n"
"v_cvt_pk_bf16_f32 v150, v84, v85\n"
"v_cvt_pk_bf16_f32 v151, v88, v89\n"
"v_pk_mul_f32 v[92:93], v[124:125], v[72:73]\n"
"v_pk_mul_f32 v[94:95], v[126:127], v[72:73]\n"
"v_pk_mul_f32 v[96:97], v[128:129], v[72:73]\n"
"v_pk_mul_f32 v[98:99], v[130:131], v[72:73]\n"
"v_pk_mul_f32 v[100:101], v[132:133], v[72:73]\n"
"v_pk_mul_f32 v[102:103], v[134:135], v[72:73]\n"
"v_pk_mul_f32 v[104:105], v[136:137], v[72:73]\n"
"v_pk_mul_f32 v[106:107], v[138:139], v[72:73]\n"
"v_pk_fma_f32 v[92:93], v[108:109], v[48:49], v[92:93]\n"
"v_pk_fma_f32 v[94:95], v[110:111], v[50:51], v[94:95]\n"
"v_pk_fma_f32 v[96:97], v[112:113], v[52:53], v[96:97]\n"
"v_pk_fma_f32 v[98:99], v[114:115], v[54:55], v[98:99]\n"
"v_pk_fma_f32 v[100:101], v[116:117], v[56:57], v[100:101]\n"
"v_pk_fma_f32 v[102:103], v[118:119], v[58:59], v[102:103]\n"
"v_pk_fma_f32 v[104:105], v[120:121], v[60:61], v[104:105]\n"
"v_pk_fma_f32 v[106:107], v[122:123], v[62:63], v[106:107]\n"
"v_cvt_pk_bf16_f32 v152, v92, v93\n"
"v_cvt_pk_bf16_f32 v153, v94, v95\n"
"v_cvt_pk_bf16_f32 v154, v96, v97\n"
"v_cvt_pk_bf16_f32 v155, v98, v99\n"
"v_cvt_pk_bf16_f32 v156, v100, v101\n"
"v_cvt_pk_bf16_f32 v157, v102, v103\n"
"v_cvt_pk_bf16_f32 v158, v104, v105\n"
"v_cvt_pk_bf16_f32 v159, v106, v107\n"
"v_cndmask_b32 v160, v12, v152, s[58:59]\n"
"v_cndmask_b32 v161, v12, v153, s[58:59]\n"
"v_cndmask_b32 v162, v12, v154, s[58:59]\n"
"v_cndmask_b32 v163, v12, v155, s[58:59]\n"
"v_cndmask_b32 v168, v152, v12, s[58:59]\n"
"v_cndmask_b32 v169, v153, v12, s[58:59]\n"
"v_cndmask_b32 v170, v154, v12, s[58:59]\n"
"v_cndmask_b32 v171, v155, v12, s[58:59]\n"
"v_mfma_f32_32x32x16_bf16 v[32:47], v[144:147], v[160:163], 0\n"
"s_nop 0\n"
"v_mfma_f32_32x32x16_bf16 v[48:63], v[144:147], v[168:171], 0\n"
"v_cndmask_b32 v164, v12, v156, s[58:59]\n"
"v_cndmask_b32 v165, v12, v157, s[58:59]\n"
"v_cndmask_b32 v166, v12, v158, s[58:59]\n"
"v_cndmask_b32 v167, v12, v159, s[58:59]\n"
"v_cndmask_b32 v172, v156, v12, s[58:59]\n"
"v_cndmask_b32 v173, v157, v12, s[58:59]\n"
"v_cndmask_b32 v174, v158, v12, s[58:59]\n"
"v_cndmask_b32 v175, v159, v12, s[58:59]\n"
"v_mfma_f32_32x32x16_bf16 v[32:47], v[148:151], v[164:167], v[32:47]\n"
"s_nop 0\n"
"v_mfma_f32_32x32x16_bf16 v[48:63], v[148:151], v[172:175], v[48:63]\n"
"s_lshl_b32 s67, s48, 2\n"
"s_add_u32 s67, s67, s45\n"
"s_mul_i32 s67, s67, 69632\n"
"s_lshl_b32 s68, s47, 7\n"
"s_add_u32 s67, s67, s68\n"
"s_add_u32 s67, s67, 1048576\n"
"s_add_u32 s68, s67, 32768\n"
"s_add_u32 s69, s67, 65536\n"
"v_lshlrev_b32 v12, 12, v2\n"
"v_lshl_add_u32 v12, v3, 4, v12\n"
"v_mov_b32 v9, s44\n"
"v_mov_b32 v89, s44\n"
"v_mov_b32 v91, s44\n"
"v_mov_b32 v93, s44\n"
"v_mov_b32 v95, s44\n"
"v_mov_b32 v97, s44\n"
"v_mov_b32 v99, s44\n"
"ds_write_b128 v15, v[32:35]\n"
"ds_write_b128 v15, v[36:39] offset:1024\n"
"ds_write_b128 v15, v[56:59] offset:2048\n"
"ds_write_b128 v15, v[60:63] offset:3072\n"
"s_waitcnt lgkmcnt(0)\n"
"s_barrier\n"
"ds_read_b64 v[76:77], v13 offset:0\n"
"ds_read_b64 v[78:79], v13 offset:8192\n"
"ds_read_b64 v[80:81], v13 offset:128\n"
"ds_read_b64 v[82:83], v13 offset:8320\n"
"ds_read_b64 v[84:85], v14 offset:0\n"
"ds_read_b64 v[86:87], v14 offset:8192\n"
"v_add_u32 v176, 73728, v180\n"
"s_waitcnt lgkmcnt(4)\n"
"v_add_f32 v88, v76, v78\n"
"v_add_f32 v90, v77, v79\n"
"buffer_store_dwordx4 v[88:91], v12, s[16:19], s67 offen sc1\n"
"s_waitcnt lgkmcnt(2)\n"
"v_add_f32 v92, v80, v82\n"
"v_add_f32 v94, v81, v83\n"
"buffer_store_dwordx4 v[92:95], v12, s[16:19], s68 offen sc1\n"
"s_waitcnt lgkmcnt(0)\n"
"v_add_f32 v96, v84, v86\n"
"v_add_f32 v98, v85, v87\n"
"s_mov_b64 exec, 0xff\n"
"buffer_store_dwordx4 v[96:99], v12, s[16:19], s69 offen sc1\n"
"s_mov_b64 exec, -1\n"
"s_waitcnt vmcnt(3)\n"
"s_waitcnt lgkmcnt(0)\n"
"s_barrier\n"
"ds_read_b64_tr_b16 v[108:109], v176 offset:0\n"
"ds_read_b64_tr_b16 v[110:111], v176 offset:512\n"
"ds_read_b64_tr_b16 v[112:113], v176 offset:2048\n"
"ds_read_b64_tr_b16 v[114:115], v176 offset:2560\n"
"ds_read_b64_tr_b16 v[116:117], v176 offset:4096\n"
"ds_read_b64_tr_b16 v[118:119], v176 offset:4608\n"
"s_waitcnt lgkmcnt(4)\n"
"v_mfma_f32_32x32x16_bf16 v[16:31], v[108:111], a[128:131], 0\n"
"ds_read_b64_tr_b16 v[120:121], v176 offset:6144\n"
"ds_read_b64_tr_b16 v[122:123], v176 offset:6656\n"
"s_add_u32 m0, s75, 122880\n"
"s_mul_i32 s7, s4, 196608\n"
"s_add_u32 s7, s7, s6\n"
"buffer_load_dwordx4 v250, s[24:27], s7 offen lds\n"
"v_mfma_f32_16x16x32_bf16 v[64:67], v[192:195], v[108:111], 0\n"
"buffer_load_dwordx4 v10, s[24:27], s7 offen offset:1024 lds\n"
"s_add_u32 s4, s4, 1\n"
"s_and_b32 s4, s4, 7\n"
"s_waitcnt lgkmcnt(4)\n"
"v_mfma_f32_32x32x16_bf16 v[16:31], v[112:115], a[132:135], v[16:31]\n"
"ds_read_b64_tr_b16 v[108:109], v176 offset:8192\n"
"ds_read_b64_tr_b16 v[110:111], v176 offset:8704\n"
"s_add_u32 m0, s75, 131072\n"
"s_mul_i32 s7, s4, 196608\n"
"s_add_u32 s7, s7, s6\n"
"buffer_load_dwordx4 v250, s[24:27], s7 offen lds\n"
"v_mfma_f32_16x16x32_bf16 v[64:67], v[192:195], v[112:115], v[64:67]\n"
"buffer_load_dwordx4 v10, s[24:27], s7 offen offset:1024 lds\n"
"s_add_u32 s4, s4, 1\n"
"s_and_b32 s4, s4, 7\n"
"s_waitcnt lgkmcnt(4)\n"
"v_mfma_f32_32x32x16_bf16 v[16:31], v[116:119], a[136:139], v[16:31]\n"
"ds_read_b64_tr_b16 v[112:113], v176 offset:10240\n"
"ds_read_b64_tr_b16 v[114:115], v176 offset:10752\n"
"s_add_u32 m0, s77, 4096\n"
"s_nop 0\n"
"buffer_load_dwordx4 v198, s[40:43], s9 offen lds\n"
"buffer_load_dwordx4 v199, s[40:43], s9 offen offset:1024 lds\n"
"v_mfma_f32_16x16x32_bf16 v[64:67], v[192:195], v[116:119], v[64:67]\n"
"s_lshr_b32 s63, s28, 7\n"
"s_sub_u32 s63, 3, s63\n"
"s_and_b32 s63, s63, 7\n"
"s_lshl_b32 s63, s63, 7\n"
"v_lshl_add_u32 v197, v2, 10, v11\n"
"s_lshl_b32 s64, s45, 17\n"
"s_lshl_b32 s65, s52, 7\n"
"s_add_u32 s64, s64, s65\n"
"s_lshl_b32 s65, s45, 14\n"
"s_waitcnt lgkmcnt(4)\n"
"v_mfma_f32_32x32x16_bf16 v[16:31], v[120:123], a[140:143], v[16:31]\n"
"ds_read_b64_tr_b16 v[116:117], v176 offset:12288\n"
"ds_read_b64_tr_b16 v[118:119], v176 offset:12800\n"
"s_lshr_b32 s66, s46, 3\n"
"s_lshl_b32 s66, s66, 2\n"
"s_add_u32 s66, s66, s45\n"
"s_lshl_b32 s76, s66, 12\n"
"v_mfma_f32_16x16x32_bf16 v[64:67], v[192:195], v[120:123], v[64:67]\n"
"s_add_u32 s63, s63, s76\n"
"v_and_b32 v15, 3, v1\n"
"v_lshlrev_b32 v15, 10, v15\n"
"buffer_load_dword v253, v15, s[28:31], s63 offen\n"
"s_waitcnt lgkmcnt(4)\n"
"v_mfma_f32_32x32x16_bf16 v[16:31], v[108:111], a[144:147], v[16:31]\n"
"ds_read_b64_tr_b16 v[120:121], v176 offset:14336\n"
"ds_read_b64_tr_b16 v[122:123], v176 offset:14848\n"
"s_lshr_b32 s63, s40, 7\n"
"s_sub_u32 s63, 3, s63\n"
"s_and_b32 s63, s63, 7\n"
"s_lshl_b32 s63, s63, 7\n"
"v_lshl_add_u32 v12, v5, 2, v187\n"
"v_lshlrev_b32 v12, 7, v12\n"
"v_lshl_add_u32 v13, v189, 3, v12\n"
"v_add_u32 v13, s77, v13\n"
"v_lshl_add_u32 v14, v188, 11, v13\n"
"v_lshl_add_u32 v251, v188, 5, v13\n"
"ds_read_b64_tr_b16 v[224:225], v14 offset:0\n"
"ds_read_b64_tr_b16 v[226:227], v14 offset:1024\n"
"ds_read_b64_tr_b16 v[228:229], v14 offset:32\n"
"ds_read_b64_tr_b16 v[230:231], v14 offset:1056\n"
"ds_read_b64_tr_b16 v[232:233], v14 offset:64\n"
"ds_read_b64_tr_b16 v[234:235], v14 offset:1088\n"
"ds_read_b64_tr_b16 v[236:237], v14 offset:96\n"
"ds_read_b64_tr_b16 v[238:239], v14 offset:1120\n"
"v_mfma_f32_16x16x32_bf16 v[64:67], v[192:195], v[108:111], v[64:67]\n"
"s_lshr_b32 s76, s66, 5\n"
"s_mul_i32 s76, s76, 28\n"
"s_bfe_u32 s10, s66, 0x20002\n"
"s_mul_i32 s10, s10, 7\n"
"s_waitcnt lgkmcnt(6)\n"
"v_mfma_f32_16x16x32_bf16 v[200:203], v[192:195], v[224:227], 0\n"
"s_add_u32 s76, s76, s10\n"
"s_bfe_u32 s10, s66, 0x10004\n"
"s_add_u32 s76, s76, s10\n"
"s_lshl_b32 s76, s76, 14\n"
"s_waitcnt lgkmcnt(4)\n"
"v_mfma_f32_16x16x32_bf16 v[204:207], v[192:195], v[228:231], 0\n"
"s_add_u32 s63, s63, s76\n"
"s_and_b32 s66, s66, 3\n"
"s_lshl_b32 s66, s66, 12\n"
"s_add_u32 s63, s63, s66\n"
"buffer_load_dword v252, v15, s[40:43], s63 offen\n"
"s_waitcnt lgkmcnt(2)\n"
"v_mfma_f32_16x16x32_bf16 v[208:211], v[192:195], v[232:235], 0\n"
"s_waitcnt lgkmcnt(0)\n"
"v_mfma_f32_16x16x32_bf16 v[212:215], v[192:195], v[236:239], 0\n"
"s_lshr_b32 s63, s47, 3\n"
"s_lshl_b32 s66, s48, 2\n"
"s_add_u32 s63, s63, s66\n"
"s_and_b32 s66, s47, 7\n"
"v_mfma_f32_32x32x16_bf16 v[16:31], v[112:115], a[148:151], v[16:31]\n"
"ds_read_b64_tr_b16 v[108:109], v176 offset:16384\n"
"ds_read_b64_tr_b16 v[110:111], v176 offset:16896\n"
"s_lshl_b32 s67, s72, 2\n"
"s_mul_i32 s68, s48, 14\n"
"s_sub_u32 s69, s67, s68\n"
"s_lshl_b32 s70, s67, 15\n"
"s_lshl_b32 s76, s66, 1\n"
"s_add_u32 s76, s76, s45\n"
"s_cmp_lt_u32 s45, 2\n"
"s_cselect_b32 s76, s76, 16\n"
"v_mfma_f32_16x16x32_bf16 v[64:67], v[192:195], v[112:115], v[64:67]\n"
"s_lshl_b32 s71, s73, 10\n"
"s_add_u32 s70, s70, s71\n"
"s_add_u32 s70, s70, 8388608\n"
"s_cmp_lt_u32 s69, 14\n"
"s_cselect_b32 s68, 0, 1073741824\n"
"s_mul_i32 s8, s63, 69632\n"
"s_lshl_b32 s10, s76, 12\n"
"s_add_u32 s8, s8, s10\n"
"s_add_u32 s8, s8, 1048576\n"
"v_mfma_f32_32x32x16_bf16 v[16:31], v[116:119], a[152:155], v[16:31]\n"
"ds_read_b64_tr_b16 v[112:113], v176 offset:18432\n"
"ds_read_b64_tr_b16 v[114:115], v176 offset:18944\n"
"s_add_u32 s69, s69, 2\n"
"s_cmp_lt_u32 s69, 14\n"
"s_cselect_b32 s69, 0, 1073741824\n"
"s_mul_i32 s9, s63, 640\n"
"s_lshl_b32 s76, s76, 5\n"
"s_add_u32 s9, s9, s76\n"
"s_add_u32 s9, s9, 458752\n"
"v_mfma_f32_16x16x32_bf16 v[64:67], v[192:195], v[116:119], v[64:67]\n"
"s_add_u32 s71, s70, s68\n"
"buffer_store_dwordx4 v[224:227], v6, s[16:19], s71 offen sc1\n"
"s_add_u32 s70, s70, 32768\n"
"s_lshl_b32 s66, s66, 2\n"
"s_add_u32 s66, s66, s45\n"
"s_cmp_lt_u32 s45, 2\n"
"s_cselect_b32 s66, 0, s66\n"
"v_mfma_f32_32x32x16_bf16 v[16:31], v[120:123], a[156:159], v[16:31]\n"
"ds_read_b64_tr_b16 v[116:117], v176 offset:20480\n"
"ds_read_b64_tr_b16 v[118:119], v176 offset:20992\n"
"s_add_u32 s71, s70, s68\n"
"buffer_store_dwordx4 v[228:231], v6, s[16:19], s71 offen sc1\n"
"s_add_u32 s70, s70, 32768\n"
"s_cmp_lt_u32 s66, 3\n"
"s_cselect_b32 s11, 0, 1\n"
"s_cselect_b32 s8, s8, 1073741824\n"
"v_lshlrev_b32 v198, 2, v1\n"
"v_mfma_f32_16x16x32_bf16 v[64:67], v[192:195], v[120:123], v[64:67]\n"
"s_add_u32 s71, s70, s69\n"
"buffer_store_dwordx4 v[232:235], v6, s[16:19], s71 offen sc1\n"
"s_add_u32 s70, s70, 32768\n"
"s_waitcnt lgkmcnt(4)\n"
"v_mfma_f32_32x32x16_bf16 v[16:31], v[108:111], a[160:163], v[16:31]\n"
"ds_read_b64_tr_b16 v[120:121], v176 offset:22528\n"
"ds_read_b64_tr_b16 v[122:123], v176 offset:23040\n"
"s_add_u32 s71, s70, s69\n"
"buffer_store_dwordx4 v[236:239], v6, s[16:19], s71 offen sc1\n"
"s_add_u32 s70, s70, 32768\n"
"v_mfma_f32_16x16x32_bf16 v[64:67], v[192:195], v[108:111], v[64:67]\n"
"s_add_u32 s67, s67, s45\n"
"s_lshl_b32 s67, s67, 7\n"
"s_and_b32 s70, s47, 7\n"
"s_mul_i32 s70, s70, 14336\n"
"s_waitcnt lgkmcnt(4)\n"
"v_mfma_f32_32x32x16_bf16 v[16:31], v[112:115], a[164:167], v[16:31]\n"
"ds_read_b64_tr_b16 v[108:109], v176 offset:24576\n"
"ds_read_b64_tr_b16 v[110:111], v176 offset:25088\n"
"s_add_u32 s67, s67, s70\n"
"s_add_u32 s67, s67, 3407872\n"
"s_cmp_lt_u32 s45, 2\n"
"s_cselect_b32 s68, s68, s69\n"
"s_add_u32 s7, s67, s68\n"
"v_mfma_f32_16x16x32_bf16 v[64:67], v[192:195], v[112:115], v[64:67]\n"
"s_add_u32 m0, s65, 0\n"
"s_nop 0\n"
"buffer_load_dwordx4 v197, s[28:31], s64 offen offset:0 lds\n"
"s_add_u32 s64, s64, 7168\n"
"v_cndmask_b32 v12, v200, v204, s[60:61]\n"
"v_cndmask_b32 v13, v208, v212, s[60:61]\n"
"v_lshlrev_b32 v14, 2, v1\n"
"v_add_u32 v14, s77, v14\n"
"ds_write_b32 v14, v12\n"
"ds_write_b32 v14, v13 offset:256\n"
"s_waitcnt lgkmcnt(6)\n"
"v_mfma_f32_32x32x16_bf16 v[16:31], v[116:119], a[168:171], v[16:31]\n"
"ds_read_b64_tr_b16 v[112:113], v176 offset:26624\n"
"ds_read_b64_tr_b16 v[114:115], v176 offset:27136\n"
"v_mfma_f32_16x16x32_bf16 v[64:67], v[192:195], v[116:119], v[64:67]\n"
"buffer_load_dwordx4 v197, s[28:31], s64 offen offset:1024 lds\n"
"s_add_u32 s64, s64, 7168\n"
"s_waitcnt lgkmcnt(6)\n"
"v_mfma_f32_32x32x16_bf16 v[16:31], v[120:123], a[172:175], v[16:31]\n"
"ds_read_b64_tr_b16 v[116:117], v176 offset:28672\n"
"ds_read_b64_tr_b16 v[118:119], v176 offset:29184\n"
"v_mfma_f32_16x16x32_bf16 v[64:67], v[192:195], v[120:123], v[64:67]\n"
"buffer_load_dwordx4 v197, s[28:31], s64 offen offset:2048 lds\n"
"s_add_u32 s64, s64, 7168\n"
"s_waitcnt lgkmcnt(6)\n"
"v_mfma_f32_32x32x16_bf16 v[16:31], v[108:111], a[176:179], v[16:31]\n"
"ds_read_b64_tr_b16 v[120:121], v176 offset:30720\n"
"ds_read_b64_tr_b16 v[122:123], v176 offset:31232\n"
"v_mfma_f32_16x16x32_bf16 v[64:67], v[192:195], v[108:111], v[64:67]\n"
"buffer_load_dwordx4 v197, s[28:31], s64 offen offset:3072 lds\n"
"s_add_u32 s64, s64, 11264\n"
"s_waitcnt lgkmcnt(4)\n"
"v_mfma_f32_32x32x16_bf16 v[16:31], v[112:115], a[180:183], v[16:31]\n"
"ds_read_b64_tr_b16 v[108:109], v176 offset:32768\n"
"ds_read_b64_tr_b16 v[110:111], v176 offset:33280\n"
"v_mfma_f32_16x16x32_bf16 v[64:67], v[192:195], v[112:115], v[64:67]\n"
"s_waitcnt lgkmcnt(4)\n"
"v_mfma_f32_32x32x16_bf16 v[16:31], v[116:119], a[184:187], v[16:31]\n"
"ds_read_b64_tr_b16 v[112:113], v176 offset:34816\n"
"ds_read_b64_tr_b16 v[114:115], v176 offset:35328\n"
"v_mfma_f32_16x16x32_bf16 v[64:67], v[192:195], v[116:119], v[64:67]\n"
"s_waitcnt lgkmcnt(4)\n"
"v_mfma_f32_32x32x16_bf16 v[16:31], v[120:123], a[188:191], v[16:31]\n"
"ds_read_b64_tr_b16 v[116:117], v176 offset:36864\n"
"ds_read_b64_tr_b16 v[118:119], v176 offset:37376\n"
"v_mfma_f32_16x16x32_bf16 v[64:67], v[192:195], v[120:123], v[64:67]\n"
"s_waitcnt lgkmcnt(4)\n"
"v_mfma_f32_32x32x16_bf16 v[16:31], v[108:111], a[192:195], v[16:31]\n"
"ds_read_b64_tr_b16 v[120:121], v176 offset:38912\n"
"ds_read_b64_tr_b16 v[122:123], v176 offset:39424\n"
"v_mfma_f32_16x16x32_bf16 v[64:67], v[192:195], v[108:111], v[64:67]\n"
"s_waitcnt lgkmcnt(4)\n"
"v_mfma_f32_32x32x16_bf16 v[16:31], v[112:115], a[196:199], v[16:31]\n"
"ds_read_b64_tr_b16 v[108:109], v176 offset:40960\n"
"ds_read_b64_tr_b16 v[110:111], v176 offset:41472\n"
"v_mfma_f32_16x16x32_bf16 v[64:67], v[192:195], v[112:115], v[64:67]\n"
"s_waitcnt lgkmcnt(4)\n"
"v_mfma_f32_32x32x16_bf16 v[16:31], v[116:119], a[200:203], v[16:31]\n"
"ds_read_b64_tr_b16 v[112:113], v176 offset:43008\n"
"ds_read_b64_tr_b16 v[114:115], v176 offset:43520\n"
"v_mfma_f32_16x16x32_bf16 v[64:67], v[192:195], v[116:119], v[64:67]\n"
"s_waitcnt lgkmcnt(4)\n"
"v_mfma_f32_32x32x16_bf16 v[16:31], v[120:123], a[204:207], v[16:31]\n"
"ds_read_b64_tr_b16 v[116:117], v176 offset:45056\n"
"ds_read_b64_tr_b16 v[118:119], v176 offset:45568\n"
"v_mfma_f32_16x16x32_bf16 v[64:67], v[192:195], v[120:123], v[64:67]\n"
"s_waitcnt lgkmcnt(4)\n"
"v_mfma_f32_32x32x16_bf16 v[16:31], v[108:111], a[208:211], v[16:31]\n"
"ds_read_b64_tr_b16 v[120:121], v176 offset:47104\n"
"ds_read_b64_tr_b16 v[122:123], v176 offset:47616\n"
"buffer_load_dwordx4 v[124:127], v6, s[16:19], s8 offen offset:0 sc1\n"
"s_waitcnt vmcnt(11)\n"
"ds_read_b64_tr_b16 v[240:241], v251 offset:4096\n"
"ds_read_b64_tr_b16 v[242:243], v251 offset:5120\n"
"v_mfma_f32_16x16x32_bf16 v[64:67], v[192:195], v[108:111], v[64:67]\n"
"buffer_load_dwordx4 v[128:131], v6, s[16:19], s8 offen offset:1024 sc1\n"
"ds_read_b64_tr_b16 v[244:245], v251 offset:4160\n"
"s_waitcnt lgkmcnt(7)\n"
"v_mfma_f32_32x32x16_bf16 v[16:31], v[112:115], a[212:215], v[16:31]\n"
"s_barrier\n"
"ds_read_b64_tr_b16 v[108:109], v176 offset:49152\n"
"ds_read_b64_tr_b16 v[110:111], v176 offset:49664\n"
"buffer_load_dwordx4 v[132:135], v6, s[16:19], s8 offen offset:2048 sc1\n"
"v_mfma_f32_16x16x32_bf16 v[64:67], v[192:195], v[112:115], v[64:67]\n"
"buffer_load_dwordx4 v[136:139], v6, s[16:19], s8 offen offset:3072 sc1\n"
"ds_read_b64_tr_b16 v[246:247], v251 offset:5184\n"
"s_waitcnt lgkmcnt(8)\n"
"v_mfma_f32_32x32x16_bf16 v[16:31], v[116:119], a[216:219], v[16:31]\n"
"ds_read_b64_tr_b16 v[112:113], v176 offset:51200\n"
"ds_read_b64_tr_b16 v[114:115], v176 offset:51712\n"
"v_mfma_f32_16x16x32_bf16 v[64:67], v[192:195], v[116:119], v[64:67]\n"
"s_waitcnt lgkmcnt(8)\n"
"v_mfma_f32_32x32x16_bf16 v[16:31], v[120:123], a[220:223], v[16:31]\n"
"ds_read_b64_tr_b16 v[116:117], v176 offset:53248\n"
"ds_read_b64_tr_b16 v[118:119], v176 offset:53760\n"
"v_mfma_f32_16x16x32_bf16 v[64:67], v[192:195], v[120:123], v[64:67]\n"
"s_waitcnt lgkmcnt(5)\n"
"v_mfma_f32_32x32x16_bf16 v[16:31], v[108:111], a[224:227], v[16:31]\n"
"ds_read_b64_tr_b16 v[120:121], v176 offset:55296\n"
"ds_read_b64_tr_b16 v[122:123], v176 offset:55808\n"
"v_mfma_f32_16x16x32_bf16 v[64:67], v[192:195], v[108:111], v[64:67]\n"
"s_waitcnt lgkmcnt(4)\n"
"v_mfma_f32_32x32x16_bf16 v[16:31], v[112:115], a[228:231], v[16:31]\n"
"ds_read_b64_tr_b16 v[108:109], v176 offset:57344\n"
"ds_read_b64_tr_b16 v[110:111], v176 offset:57856\n"
"v_mfma_f32_16x16x32_bf16 v[64:67], v[192:195], v[112:115], v[64:67]\n"
"s_waitcnt lgkmcnt(4)\n"
"v_mfma_f32_32x32x16_bf16 v[16:31], v[116:119], a[232:235], v[16:31]\n"
"ds_read_b64_tr_b16 v[112:113], v176 offset:59392\n"
"ds_read_b64_tr_b16 v[114:115], v176 offset:59904\n"
"v_mfma_f32_16x16x32_bf16 v[64:67], v[192:195], v[116:119], v[64:67]\n"
"s_waitcnt lgkmcnt(4)\n"
"v_mfma_f32_32x32x16_bf16 v[16:31], v[120:123], a[236:239], v[16:31]\n"
"ds_read_b64_tr_b16 v[116:117], v176 offset:61440\n"
"ds_read_b64_tr_b16 v[118:119], v176 offset:61952\n"
"v_mfma_f32_16x16x32_bf16 v[64:67], v[192:195], v[120:123], v[64:67]\n"
"s_waitcnt lgkmcnt(4)\n"
"v_mfma_f32_32x32x16_bf16 v[16:31], v[108:111], a[240:243], v[16:31]\n"
"ds_read_b64_tr_b16 v[120:121], v176 offset:63488\n"
"ds_read_b64_tr_b16 v[122:123], v176 offset:64000\n"
"v_mfma_f32_16x16x32_bf16 v[64:67], v[192:195], v[108:111], v[64:67]\n"
"s_waitcnt lgkmcnt(4)\n"
"v_mfma_f32_16x16x32_bf16 v[64:67], v[192:195], v[112:115], v[64:67]\n"
"s_waitcnt lgkmcnt(2)\n"
"v_mfma_f32_16x16x32_bf16 v[64:67], v[192:195], v[116:119], v[64:67]\n"
"s_waitcnt lgkmcnt(0)\n"
"v_mfma_f32_16x16x32_bf16 v[64:67], v[192:195], v[120:123], v[64:67]\n"
"v_mfma_f32_32x32x16_bf16 v[16:31], v[112:115], a[244:247], v[16:31]\n"
"s_lshl_b32 s69, s45, 9\n"
"s_add_u32 s69, s69, 65792\n"
"v_lshlrev_b32 v12, 4, v5\n"
"v_add_u32 v12, s69, v12\n"
"v_mfma_f32_32x32x16_bf16 v[16:31], v[116:119], a[248:251], v[16:31]\n"
"s_nop 1\n"
"ds_write_b32 v249, v64 offset:256\n"
"ds_read_b128 v[200:203], v12 offset:0\n"
"ds_read_b128 v[204:207], v12 offset:32\n"
"ds_read_b128 v[208:211], v12 offset:64\n"
"ds_read_b128 v[212:215], v12 offset:96\n"
"v_mfma_f32_32x32x16_bf16 v[16:31], v[120:123], a[252:255], v[16:31]\n"
"s_waitcnt lgkmcnt(0)\n"
"s_barrier\n"
"s_cmp_eq_u32 s11, 0\n"
"s_cbranch_scc0 .LK_h1skip_3\n"
"s_waitcnt vmcnt(3)\n"
"v_sad_u32 v13, v125, s44, 0\n"
"s_waitcnt vmcnt(3)\n"
"v_sad_u32 v13, v127, s44, v13\n"
"s_waitcnt vmcnt(2)\n"
"v_sad_u32 v13, v129, s44, v13\n"
"s_waitcnt vmcnt(2)\n"
"v_sad_u32 v13, v131, s44, v13\n"
"s_waitcnt vmcnt(1)\n"
"v_sad_u32 v13, v133, s44, v13\n"
"s_waitcnt vmcnt(1)\n"
"v_sad_u32 v13, v135, s44, v13\n"
"s_waitcnt vmcnt(0)\n"
"v_sad_u32 v13, v137, s44, v13\n"
"s_waitcnt vmcnt(0)\n"
"v_sad_u32 v13, v139, s44, v13\n"
"v_cmp_ne_u32 vcc, 0, v13\n"
"s_cbranch_vccz .LK_h1ok_4\n"
"s_mov_b32 s53, 32768\n"
".LK_h1sp_5:\n"
"s_waitcnt vmcnt(3)\n"
"buffer_load_dwordx4 v[124:127], v6, s[16:19], s8 offen offset:0 sc1\n"
"s_waitcnt vmcnt(2)\n"
"buffer_load_dwordx4 v[128:131], v6, s[16:19], s8 offen offset:1024 sc1\n"
"s_waitcnt vmcnt(1)\n"
"buffer_load_dwordx4 v[132:135], v6, s[16:19], s8 offen offset:2048 sc1\n"
"s_waitcnt vmcnt(0)\n"
"buffer_load_dwordx4 v[136:139], v6, s[16:19], s8 offen offset:3072 sc1\n"
"s_waitcnt vmcnt(0)\n"
"s_waitcnt vmcnt(3)\n"
"v_sad_u32 v13, v125, s44, 0\n"
"s_waitcnt vmcnt(3)\n"
"v_sad_u32 v13, v127, s44, v13\n"
"s_waitcnt vmcnt(2)\n"
"v_sad_u32 v13, v129, s44, v13\n"
"s_waitcnt vmcnt(2)\n"
"v_sad_u32 v13, v131, s44, v13\n"
"s_waitcnt vmcnt(1)\n"
"v_sad_u32 v13, v133, s44, v13\n"
"s_waitcnt vmcnt(1)\n"
"v_sad_u32 v13, v135, s44, v13\n"
"s_waitcnt vmcnt(0)\n"
"v_sad_u32 v13, v137, s44, v13\n"
"s_waitcnt vmcnt(0)\n"
"v_sad_u32 v13, v139, s44, v13\n"
"v_cmp_ne_u32 vcc, 0, v13\n"
"s_cbranch_vccz .LK_h1ok_4\n"
"s_sub_u32 s53, s53, 1\n"
"s_cbranch_scc0 .LK_h1sp_5\n"
".LK_h1ok_4:\n"
"s_waitcnt vmcnt(2)\n"
"v_add_f32 v124, v124, v128\n"
"s_waitcnt vmcnt(0)\n"
"v_add_f32 v132, v132, v136\n"
"s_waitcnt vmcnt(2)\n"
"v_add_f32 v126, v126, v130\n"
"s_waitcnt vmcnt(0)\n"
"v_add_f32 v134, v134, v138\n"
"s_waitcnt vmcnt(1)\n"
"v_add_f32 v124, v124, v132\n"
"s_waitcnt vmcnt(1)\n"
"v_add_f32 v126, v126, v134\n"
"s_waitcnt vmcnt(3)\n"
"v_add_f32_dpp v124, v124, v124 row_ror:8 row_mask:0xf bank_mask:0xf\n"
"s_waitcnt vmcnt(3)\n"
"v_add_f32_dpp v126, v126, v126 row_ror:8 row_mask:0xf bank_mask:0xf\n"
"s_waitcnt vmcnt(3)\n"
"v_mov_b32 v125, v124\n"
"s_waitcnt vmcnt(3)\n"
"v_mov_b32 v127, v126\n"
"s_waitcnt vmcnt(3)\n"
"v_permlane16_swap_b32 v124, v125\n"
"s_waitcnt vmcnt(3)\n"
"v_permlane16_swap_b32 v126, v127\n"
"s_waitcnt vmcnt(3)\n"
"v_add_f32 v124, v124, v125\n"
"s_waitcnt vmcnt(3)\n"
"v_add_f32 v126, v126, v127\n"
"s_waitcnt vmcnt(3)\n"
"v_mov_b32 v125, v124\n"
"s_waitcnt vmcnt(3)\n"
"v_mov_b32 v127, v126\n"
"s_waitcnt vmcnt(3)\n"
"v_permlane32_swap_b32 v124, v125\n"
"s_waitcnt vmcnt(3)\n"
"v_permlane32_swap_b32 v126, v127\n"
"s_waitcnt vmcnt(3)\n"
"v_add_f32 v124, v124, v125\n"
"s_waitcnt vmcnt(3)\n"
"v_add_f32 v126, v126, v127\n"
"s_waitcnt vmcnt(3)\n"
"v_cvt_pk_bf16_f32 v124, v124, v126\n"
"s_mov_b64 exec, 0xff\n"
"s_waitcnt vmcnt(3)\n"
"buffer_store_dword v124, v198, s[16:19], s9 offen sc1\n"
"s_mov_b64 exec, -1\n"
".LK_h1skip_3:\n"
"s_lshl_b32 s69, s45, 7\n"
"s_add_u32 s69, s69, 139264\n"
"v_lshlrev_b32 v12, 2, v186\n"
"v_add_u32 v12, s69, v12\n"
"ds_read_b32 v108, v12 offset:0\n"
"ds_read_b32 v109, v12 offset:6144\n"
"ds_read_b32 v110, v12 offset:12288\n"
"ds_read_b32 v111, v12 offset:18432\n"
"s_lshl_b32 s67, s47, 1\n"
"s_lshr_b32 s68, s45, 1\n"
"s_add_u32 s67, s67, s68\n"
"s_lshl_b32 s67, s67, 15\n"
"s_lshl_b32 s68, s48, 1\n"
"s_and_b32 s69, s45, 1\n"
"s_add_u32 s68, s68, s69\n"
"s_lshl_b32 s68, s68, 11\n"
"s_add_u32 s67, s67, s68\n"
"s_add_u32 s67, s67, 4194304\n"
"v_pk_fma_f32 v[76:77], v[142:143], v[200:201], s[54:55] op_sel_hi:[0,1,1]\n"
"v_pk_fma_f32 v[80:81], v[142:143], v[202:203], s[54:55] op_sel_hi:[0,1,1]\n"
"v_pk_fma_f32 v[84:85], v[142:143], v[204:205], s[54:55] op_sel_hi:[0,1,1]\n"
"v_pk_fma_f32 v[88:89], v[142:143], v[206:207], s[54:55] op_sel_hi:[0,1,1]\n"
"v_pk_fma_f32 v[76:77], v[140:141], v[16:17], v[76:77] op_sel_hi:[0,1,1]\n"
"v_pk_fma_f32 v[80:81], v[140:141], v[18:19], v[80:81] op_sel_hi:[0,1,1]\n"
"v_pk_fma_f32 v[84:85], v[140:141], v[20:21], v[84:85] op_sel_hi:[0,1,1]\n"
"v_pk_fma_f32 v[88:89], v[140:141], v[22:23], v[88:89] op_sel_hi:[0,1,1]\n"
"v_pk_mul_f32 v[78:79], v[76:77], s[56:57]\n"
"v_pk_mul_f32 v[82:83], v[80:81], s[56:57]\n"
"v_pk_mul_f32 v[86:87], v[84:85], s[56:57]\n"
"v_pk_mul_f32 v[90:91], v[88:89], s[56:57]\n"
"v_exp_f32_e64 v78, v78 clamp\n"
"v_max_f32 v76, s54, v76\n"
"v_exp_f32_e64 v79, v79 clamp\n"
"v_max_f32 v77, s54, v77\n"
"v_exp_f32_e64 v82, v82 clamp\n"
"v_max_f32 v80, s54, v80\n"
"v_exp_f32_e64 v83, v83 clamp\n"
"v_max_f32 v81, s54, v81\n"
"v_exp_f32_e64 v86, v86 clamp\n"
"v_max_f32 v84, s54, v84\n"
"v_exp_f32_e64 v87, v87 clamp\n"
"v_max_f32 v85, s54, v85\n"
"v_exp_f32_e64 v90, v90 clamp\n"
"v_max_f32 v88, s54, v88\n"
"v_exp_f32_e64 v91, v91 clamp\n"
"v_max_f32 v89, s54, v89\n"
"v_pk_add_f32 v[76:77], v[76:77], v[78:79]\n"
"v_pk_add_f32 v[80:81], v[80:81], v[82:83]\n"
"v_pk_add_f32 v[84:85], v[84:85], v[86:87]\n"
"v_pk_add_f32 v[88:89], v[88:89], v[90:91]\n"
"v_cvt_pk_bf16_f32 v216, v76, v77\n"
"v_cvt_pk_bf16_f32 v217, v80, v81\n"
"v_cvt_pk_bf16_f32 v218, v84, v85\n"
"v_cvt_pk_bf16_f32 v219, v88, v89\n"
"v_pk_fma_f32 v[76:77], v[142:143], v[208:209], s[54:55] op_sel_hi:[0,1,1]\n"
"v_pk_fma_f32 v[80:81], v[142:143], v[210:211], s[54:55] op_sel_hi:[0,1,1]\n"
"v_pk_fma_f32 v[84:85], v[142:143], v[212:213], s[54:55] op_sel_hi:[0,1,1]\n"
"v_pk_fma_f32 v[88:89], v[142:143], v[214:215], s[54:55] op_sel_hi:[0,1,1]\n"
"v_pk_fma_f32 v[76:77], v[140:141], v[24:25], v[76:77] op_sel_hi:[0,1,1]\n"
"v_pk_fma_f32 v[80:81], v[140:141], v[26:27], v[80:81] op_sel_hi:[0,1,1]\n"
"v_pk_fma_f32 v[84:85], v[140:141], v[28:29], v[84:85] op_sel_hi:[0,1,1]\n"
"v_pk_fma_f32 v[88:89], v[140:141], v[30:31], v[88:89] op_sel_hi:[0,1,1]\n"
"v_pk_mul_f32 v[78:79], v[76:77], s[56:57]\n"
"v_pk_mul_f32 v[82:83], v[80:81], s[56:57]\n"
"v_pk_mul_f32 v[86:87], v[84:85], s[56:57]\n"
"v_pk_mul_f32 v[90:91], v[88:89], s[56:57]\n"
"v_exp_f32_e64 v78, v78 clamp\n"
"v_max_f32 v76, s54, v76\n"
"v_exp_f32_e64 v79, v79 clamp\n"
"v_max_f32 v77, s54, v77\n"
"v_exp_f32_e64 v82, v82 clamp\n"
"v_max_f32 v80, s54, v80\n"
"v_exp_f32_e64 v83, v83 clamp\n"
"v_max_f32 v81, s54, v81\n"
"v_exp_f32_e64 v86, v86 clamp\n"
"v_max_f32 v84, s54, v84\n"
"v_exp_f32_e64 v87, v87 clamp\n"
"v_max_f32 v85, s54, v85\n"
"v_exp_f32_e64 v90, v90 clamp\n"
"v_max_f32 v88, s54, v88\n"
"v_exp_f32_e64 v91, v91 clamp\n"
"v_max_f32 v89, s54, v89\n"
"v_pk_add_f32 v[76:77], v[76:77], v[78:79]\n"
"v_pk_add_f32 v[80:81], v[80:81], v[82:83]\n"
"v_pk_add_f32 v[84:85], v[84:85], v[86:87]\n"
"v_pk_add_f32 v[88:89], v[88:89], v[90:91]\n"
"v_cvt_pk_bf16_f32 v220, v76, v77\n"
"v_cvt_pk_bf16_f32 v221, v80, v81\n"
"v_cvt_pk_bf16_f32 v222, v84, v85\n"
"v_cvt_pk_bf16_f32 v223, v88, v89\n"
"buffer_store_dwordx4 v[216:219], v6, s[16:19], s67 offen sc1\n"
"buffer_store_dwordx4 v[220:223], v6, s[16:19], s67 offen offset:1024 sc1\n"
"s_mul_i32 s70, s48, 229376\n"
"s_lshl_b32 s71, s74, 11\n"
"s_add_u32 s70, s70, s71\n"
"s_add_u32 s70, s70, 12582912\n"
"s_cmp_lt_u32 s74, 112\n"
"s_cselect_b32 s70, s70, 1073741824\n"
"buffer_store_dwordx4 v[240:243], v6, s[16:19], s70 offen sc1\n"
"buffer_store_dwordx4 v[244:247], v6, s[16:19], s70 offen offset:1024 sc1\n"
"s_waitcnt lgkmcnt(2)\n"
"v_add_f32 v108, v108, v109\n"
"s_waitcnt lgkmcnt(0)\n"
"v_add_f32 v110, v110, v111\n"
"v_add_f32 v248, v108, v110\n"
"v_lshlrev_b32 v12, 2, v186\n"
"buffer_store_dword v248, v12, s[16:19], s7 offen sc1\n"
"s_lshl_b32 s66, s51, 1\n"
"s_lshr_b32 s67, s45, 1\n"
"s_add_u32 s66, s66, s67\n"
"s_lshl_b32 s76, s66, 15\n"
"s_lshl_b32 s67, s52, 7\n"
"s_add_u32 s76, s76, s67\n"
"s_and_b32 s67, s45, 1\n"
"s_lshl_b32 s68, s67, 6\n"
"s_add_u32 s76, s76, s68\n"
"v_lshlrev_b32 v11, 10, v186\n"
"v_lshl_add_u32 v10, v5, 4, v11\n"
"s_add_u32 m0, s65, 4096\n"
"s_nop 0\n"
"buffer_load_dwordx4 v197, s[28:31], s64 offen offset:0 lds\n"
"s_add_u32 s64, s64, 7168\n"
"buffer_load_dwordx4 v197, s[28:31], s64 offen offset:1024 lds\n"
"s_add_u32 s64, s64, 7168\n"
"buffer_load_dwordx4 v197, s[28:31], s64 offen offset:2048 lds\n"
"s_add_u32 s64, s64, 7168\n"
"buffer_load_dwordx4 v197, s[28:31], s64 offen offset:3072 lds\n"
"s_add_u32 s64, s64, 11264\n"
"s_add_u32 m0, s65, 8192\n"
"s_nop 0\n"
"buffer_load_dwordx4 v197, s[28:31], s64 offen offset:0 lds\n"
"s_add_u32 s64, s64, 7168\n"
"buffer_load_dwordx4 v197, s[28:31], s64 offen offset:1024 lds\n"
"s_add_u32 s64, s64, 7168\n"
"v_xor_b32 v12, s67, v191\n"
"v_lshl_add_u32 v13, v5, 2, v187\n"
"v_lshl_add_u32 v13, v13, 7, v190\n"
"s_lshl_b32 s67, s45, 14\n"
"v_add_u32 v13, s67, v13\n"
"v_lshl_add_u32 v236, v12, 6, v13\n"
"v_xor_b32 v12, 1, v12\n"
"v_lshl_add_u32 v237, v12, 6, v13\n"
"v_lshlrev_b32 v12, 15, v1\n"
"s_lshl_b32 s67, s46, 7\n"
"s_add_u32 s67, s67, 786432\n"
"s_cmp_eq_u32 s45, 0\n"
"s_cselect_b32 s67, s67, 1073741824\n"
"s_and_b32 s8, s46, 7\n"
"s_lshl_b32 s8, s8, 15\n"
"s_lshl_b32 s68, s45, 13\n"
"s_add_u32 s8, s8, s68\n"
"s_add_u32 s8, s8, 786432\n"
"v_lshlrev_b32 v11, 7, v1\n"
"s_waitcnt vmcnt(6)\n"
"s_barrier\n"
"s_mov_b64 exec, 0xff\n"
"buffer_store_dword v9, v12, s[16:19], s67 offen sc1\n"
"s_mov_b64 exec, -1\n"
"buffer_load_dwordx4 v197, s[28:31], s64 offen offset:2048 lds\n"
"s_add_u32 s64, s64, 7168\n"
"buffer_load_dwordx4 v197, s[28:31], s64 offen offset:3072 lds\n"
"s_add_u32 s64, s64, 11264\n"
"s_add_u32 m0, s65, 12288\n"
"s_nop 0\n"
"buffer_load_dwordx4 v197, s[28:31], s64 offen offset:0 lds\n"
"s_add_u32 s64, s64, 7168\n"
"buffer_load_dwordx4 v197, s[28:31], s64 offen offset:1024 lds\n"
"s_add_u32 s64, s64, 7168\n"
"buffer_load_dwordx4 v197, s[28:31], s64 offen offset:2048 lds\n"
"s_add_u32 s64, s64, 7168\n"
"buffer_load_dwordx4 v197, s[28:31], s64 offen offset:3072 lds\n"
"s_add_u32 s64, s64, 11264\n"
"buffer_load_dwordx4 v[208:211], v10, s[20:23], s76 offen\n"
"buffer_load_dwordx4 v[212:215], v10, s[20:23], s76 offen offset:32\n"
"s_mul_i32 s9, s45, 458752\n"
"s_lshl_b32 s67, s52, 7\n"
"s_add_u32 s9, s9, s67\n"
"s_mul_i32 s10, s45, 24576\n"
"s_add_u32 s10, s10, 65536\n"
"v_add_u32 v238, 7168, v197\n"
"s_lshl_b32 s67, s45, 14\n"
"s_sub_u32 s67, s10, s67\n"
"v_add_u32 v239, s67, v236\n"
"v_add_u32 v240, s67, v237\n"
"s_add_u32 m0, s10, 0\n"
"s_add_u32 s67, s9, 0\n"
"buffer_load_dwordx4 v197, s[40:43], s67 offen lds\n"
"buffer_load_dwordx4 v238, s[40:43], s67 offen offset:1024 lds\n"
"s_add_u32 m0, s10, 2048\n"
"s_add_u32 s67, s9, 114688\n"
"buffer_load_dwordx4 v197, s[40:43], s67 offen lds\n"
"buffer_load_dwordx4 v238, s[40:43], s67 offen offset:1024 lds\n"
"s_add_u32 m0, s10, 4096\n"
"s_add_u32 s67, s9, 229376\n"
"buffer_load_dwordx4 v197, s[40:43], s67 offen lds\n"
"buffer_load_dwordx4 v238, s[40:43], s67 offen offset:1024 lds\n"
"s_add_u32 m0, s10, 6144\n"
"s_add_u32 s67, s9, 344064\n"
"buffer_load_dwordx4 v197, s[40:43], s67 offen lds\n"
"buffer_load_dwordx4 v238, s[40:43], s67 offen offset:1024 lds\n"
"s_add_u32 m0, s10, 8192\n"
"s_add_u32 s67, s9, 16384\n"
"buffer_load_dwordx4 v197, s[40:43], s67 offen lds\n"
"buffer_load_dwordx4 v238, s[40:43], s67 offen offset:1024 lds\n"
"s_add_u32 m0, s10, 10240\n"
"s_add_u32 s67, s9, 131072\n"
"buffer_load_dwordx4 v197, s[40:43], s67 offen lds\n"
"buffer_load_dwordx4 v238, s[40:43], s67 offen offset:1024 lds\n"
"s_add_u32 m0, s10, 12288\n"
"s_add_u32 s67, s9, 245760\n"
"buffer_load_dwordx4 v197, s[40:43], s67 offen lds\n"
"buffer_load_dwordx4 v238, s[40:43], s67 offen offset:1024 lds\n"
"s_add_u32 m0, s10, 14336\n"
"s_add_u32 s67, s9, 360448\n"
"buffer_load_dwordx4 v197, s[40:43], s67 offen lds\n"
"buffer_load_dwordx4 v238, s[40:43], s67 offen offset:1024 lds\n"
"s_mul_i32 s69, s45, 5120\n"
"s_add_u32 s69, s69, 458752\n"
"s_add_u32 s70, s69, 2560\n"
"v_min_u32 v13, 16, v186\n"
"v_lshlrev_b32 v13, 5, v13\n"
"v_lshl_add_u32 v13, v5, 4, v13\n"
"v_xor_b32 v12, 16, v186\n"
"v_min_u32 v14, 16, v12\n"
"v_lshlrev_b32 v14, 5, v14\n"
"v_lshl_add_u32 v14, v5, 4, v14\n"
"s_and_b32 s5, s46, 6\n"
"s_lshl_b32 s75, s45, 13\n"
"s_mul_i32 s67, s49, 229376\n"
"s_add_u32 s67, s67, 8388608\n"
"s_lshl_b32 s68, s45, 1\n"
"s_add_u32 s68, s68, s5\n"
"s_and_b32 s71, s68, 7\n"
"s_lshl_b32 s71, s71, 12\n"
"s_add_u32 s63, s67, s71\n"
"s_add_u32 s68, s68, 1\n"
"s_and_b32 s71, s68, 7\n"
"s_lshl_b32 s71, s71, 12\n"
"s_add_u32 s4, s67, s71\n"
"s_add_u32 s68, s68, 1\n"
"s_mov_b32 s24, s78\n"
"s_and_b32 s25, s79, 0xffff\n"
"s_mov_b32 s26, 0x200000\n"
"s_mov_b32 s27, 0x20000\n"
"s_xor_b32 s77, s45, 1\n"
"s_mul_i32 s77, s77, 12288\n"
"s_add_u32 s77, s77, 0\n"
"v_add_u32 v232, s77, v6\n"
"s_xor_b32 s77, s45, 2\n"
"s_mul_i32 s77, s77, 12288\n"
"s_add_u32 s77, s77, 4096\n"
"v_add_u32 v233, s77, v6\n"
"s_xor_b32 s77, s45, 3\n"
"s_mul_i32 s77, s77, 12288\n"
"s_add_u32 s77, s77, 8192\n"
"v_add_u32 v234, s77, v6\n"
"s_mul_i32 s77, s45, 12288\n"
"s_add_u32 s77, s77, 0\n"
"v_add_u32 v235, s77, v6\n"
"s_lshl_b32 s72, s66, 15\n"
"s_add_u32 s72, s72, s75\n"
"s_add_u32 s72, s72, 4194304\n"
"s_xor_b32 s73, s72, 32768\n"
"s_mul_i32 s6, s45, 28672\n"
"s_mul_i32 s68, s49, 896\n"
"s_add_u32 s6, s6, s68\n"
"s_add_u32 s6, s6, 3407872\n"
"s_add_u32 s7, s6, 14336\n"
"s_waitcnt vmcnt(18)\n"
"ds_read_b64_tr_b16 a[0:1], v236 offset:0\n"
"ds_read_b64_tr_b16 a[2:3], v236 offset:1024\n"
"ds_read_b64_tr_b16 a[4:5], v237 offset:0\n"
"ds_read_b64_tr_b16 a[6:7], v237 offset:1024\n"
"ds_read_b64_tr_b16 a[8:9], v236 offset:2048\n"
"ds_read_b64_tr_b16 a[10:11], v236 offset:3072\n"
"ds_read_b64_tr_b16 a[12:13], v237 offset:2048\n"
"ds_read_b64_tr_b16 a[14:15], v237 offset:3072\n"
"ds_read_b64_tr_b16 a[16:17], v236 offset:4096\n"
"ds_read_b64_tr_b16 a[18:19], v236 offset:5120\n"
"ds_read_b64_tr_b16 a[20:21], v237 offset:4096\n"
"ds_read_b64_tr_b16 a[22:23], v237 offset:5120\n"
"ds_read_b64_tr_b16 a[24:25], v236 offset:6144\n"
"ds_read_b64_tr_b16 a[26:27], v236 offset:7168\n"
"ds_read_b64_tr_b16 a[28:29], v237 offset:6144\n"
"ds_read_b64_tr_b16 a[30:31], v237 offset:7168\n"
"ds_read_b64_tr_b16 a[32:33], v236 offset:8192\n"
"ds_read_b64_tr_b16 a[34:35], v236 offset:9216\n"
"ds_read_b64_tr_b16 a[36:37], v237 offset:8192\n"
"ds_read_b64_tr_b16 a[38:39], v237 offset:9216\n"
"ds_read_b64_tr_b16 a[40:41], v236 offset:10240\n"
"ds_read_b64_tr_b16 a[42:43], v236 offset:11264\n"
"ds_read_b64_tr_b16 a[44:45], v237 offset:10240\n"
"ds_read_b64_tr_b16 a[46:47], v237 offset:11264\n"
"ds_read_b64_tr_b16 a[48:49], v236 offset:12288\n"
"ds_read_b64_tr_b16 a[50:51], v236 offset:13312\n"
"ds_read_b64_tr_b16 a[52:53], v237 offset:12288\n"
"ds_read_b64_tr_b16 a[54:55], v237 offset:13312\n"
"ds_read_b64_tr_b16 a[56:57], v236 offset:14336\n"
"ds_read_b64_tr_b16 a[58:59], v236 offset:15360\n"
"ds_read_b64_tr_b16 a[60:61], v237 offset:14336\n"
"ds_read_b64_tr_b16 a[62:63], v237 offset:15360\n"
"s_waitcnt vmcnt(17)\n"
"v_permlane32_swap_b32 v208, v210\n"
"v_permlane32_swap_b32 v209, v211\n"
"s_waitcnt vmcnt(16)\n"
"v_permlane32_swap_b32 v212, v214\n"
"v_permlane32_swap_b32 v213, v215\n"
"v_lshlrev_b32 v216, 16, v208\n"
"v_and_b32 v217, 0xffff0000, v208\n"
"v_lshlrev_b32 v218, 16, v209\n"
"v_and_b32 v219, 0xffff0000, v209\n"
"v_lshlrev_b32 v220, 16, v210\n"
"v_and_b32 v221, 0xffff0000, v210\n"
"v_lshlrev_b32 v222, 16, v211\n"
"v_and_b32 v223, 0xffff0000, v211\n"
"v_lshlrev_b32 v224, 16, v212\n"
"v_and_b32 v225, 0xffff0000, v212\n"
"v_lshlrev_b32 v226, 16, v213\n"
"v_and_b32 v227, 0xffff0000, v213\n"
"v_lshlrev_b32 v228, 16, v214\n"
"v_and_b32 v229, 0xffff0000, v214\n"
"v_lshlrev_b32 v230, 16, v215\n"
"v_and_b32 v231, 0xffff0000, v215\n"
"s_waitcnt vmcnt(12)\n"
"ds_read_b64_tr_b16 a[128:129], v239 offset:0\n"
"ds_read_b64_tr_b16 a[130:131], v239 offset:1024\n"
"ds_read_b64_tr_b16 a[132:133], v240 offset:0\n"
"ds_read_b64_tr_b16 a[134:135], v240 offset:1024\n"
"ds_read_b64_tr_b16 a[136:137], v239 offset:2048\n"
"ds_read_b64_tr_b16 a[138:139], v239 offset:3072\n"
"ds_read_b64_tr_b16 a[140:141], v240 offset:2048\n"
"ds_read_b64_tr_b16 a[142:143], v240 offset:3072\n"
"s_waitcnt vmcnt(8)\n"
"ds_read_b64_tr_b16 a[144:145], v239 offset:4096\n"
"ds_read_b64_tr_b16 a[146:147], v239 offset:5120\n"
"ds_read_b64_tr_b16 a[148:149], v240 offset:4096\n"
"ds_read_b64_tr_b16 a[150:151], v240 offset:5120\n"
"ds_read_b64_tr_b16 a[152:153], v239 offset:6144\n"
"ds_read_b64_tr_b16 a[154:155], v239 offset:7168\n"
"ds_read_b64_tr_b16 a[156:157], v240 offset:6144\n"
"ds_read_b64_tr_b16 a[158:159], v240 offset:7168\n"
"s_waitcnt vmcnt(4)\n"
"ds_read_b64_tr_b16 a[160:161], v239 offset:8192\n"
"ds_read_b64_tr_b16 a[162:163], v239 offset:9216\n"
"ds_read_b64_tr_b16 a[164:165], v240 offset:8192\n"
"ds_read_b64_tr_b16 a[166:167], v240 offset:9216\n"
"ds_read_b64_tr_b16 a[168:169], v239 offset:10240\n"
"ds_read_b64_tr_b16 a[170:171], v239 offset:11264\n"
"ds_read_b64_tr_b16 a[172:173], v240 offset:10240\n"
"ds_read_b64_tr_b16 a[174:175], v240 offset:11264\n"
"s_waitcnt vmcnt(0)\n"
"ds_read_b64_tr_b16 a[176:177], v239 offset:12288\n"
"ds_read_b64_tr_b16 a[178:179], v239 offset:13312\n"
"ds_read_b64_tr_b16 a[180:181], v240 offset:12288\n"
"ds_read_b64_tr_b16 a[182:183], v240 offset:13312\n"
"ds_read_b64_tr_b16 a[184:185], v239 offset:14336\n"
"ds_read_b64_tr_b16 a[186:187], v239 offset:15360\n"
"ds_read_b64_tr_b16 a[188:189], v240 offset:14336\n"
"ds_read_b64_tr_b16 a[190:191], v240 offset:15360\n"
"s_mov_b32 s53, 32768\n"
".LK_ga_6:\n"
"buffer_load_dword v12, v11, s[16:19], s8 offen sc1\n"
"s_waitcnt vmcnt(0)\n"
"v_cmp_ne_u32 vcc, s44, v12\n"
"s_cbranch_vccz .LK_gax_7\n"
"s_sub_u32 s53, s53, 1\n"
"s_cbranch_scc0 .LK_ga_6\n"
".LK_gax_7:\n"
"s_waitcnt lgkmcnt(0)\n"
"s_barrier\n"
"buffer_load_dwordx4 v[80:83], v13, s[16:19], s69 offen offset:0 sc1\n"
"buffer_load_dwordx4 a[96:99], v6, s[16:19], s73 offen offset:0 sc1\n"
"buffer_load_dwordx4 v[84:87], v14, s[16:19], s69 offen offset:640 sc1\n"
"buffer_load_dwordx4 a[100:103], v6, s[16:19], s73 offen offset:1024 sc1\n"
"buffer_load_dwordx4 v[88:91], v13, s[16:19], s69 offen offset:1280 sc1\n"
"buffer_load_dwordx4 a[104:107], v6, s[16:19], s73 offen offset:2048 sc1\n"
"buffer_load_dwordx4 v[92:95], v14, s[16:19], s69 offen offset:1920 sc1\n"
"buffer_load_dwordx4 a[108:111], v6, s[16:19], s73 offen offset:3072 sc1\n"
"buffer_load_dwordx4 v[96:99], v13, s[16:19], s70 offen offset:0 sc1\n"
"s_add_u32 s73, s73, 4096\n"
"buffer_load_dwordx4 a[112:115], v6, s[16:19], s73 offen offset:0 sc1\n"
"buffer_load_dwordx4 v[100:103], v14, s[16:19], s70 offen offset:640 sc1\n"
"buffer_load_dwordx4 a[116:119], v6, s[16:19], s73 offen offset:1024 sc1\n"
"buffer_load_dwordx4 v[104:107], v13, s[16:19], s70 offen offset:1280 sc1\n"
"buffer_load_dwordx4 a[120:123], v6, s[16:19], s73 offen offset:2048 sc1\n"
"buffer_load_dwordx4 v[108:111], v14, s[16:19], s70 offen offset:1920 sc1\n"
"buffer_load_dwordx4 a[124:127], v6, s[16:19], s73 offen offset:3072 sc1\n"
"s_waitcnt vmcnt(14)\n"
"v_mfma_f32_32x32x16_bf16 v[144:159], v[80:83], a[96:99], 0\n"
"s_waitcnt vmcnt(12)\n"
"v_mfma_f32_32x32x16_bf16 v[160:175], v[84:87], a[100:103], 0\n"
"buffer_load_dwordx4 a[64:67], v6, s[16:19], s72 offen offset:0 sc1\n"
"s_nop 8\n"
"v_add_f32 v11, s54, v152\n"
"v_rcp_f32 v11, v11\n"
"s_nop 0\n"
"v_mul_f32 v144, v144, v11\n"
"v_mul_f32 v145, v145, v11\n"
"v_mul_f32 v146, v146, v11\n"
"v_mul_f32 v147, v147, v11\n"
"v_mul_f32 v148, v148, v11\n"
"v_mul_f32 v149, v149, v11\n"
"v_mul_f32 v150, v150, v11\n"
"v_mul_f32 v151, v151, v11\n"
"v_cvt_pk_bf16_f32 v176, v144, v145\n"
"v_cvt_pk_bf16_f32 v177, v146, v147\n"
"v_cvt_pk_bf16_f32 v178, v148, v149\n"
"v_cvt_pk_bf16_f32 v179, v150, v151\n"
"buffer_load_dwordx4 a[68:71], v6, s[16:19], s72 offen offset:1024 sc1\n"
"s_waitcnt vmcnt(12)\n"
"v_mfma_f32_32x32x16_bf16 v[144:159], v[88:91], a[104:107], 0\n"
"buffer_load_dwordx4 a[72:75], v6, s[16:19], s72 offen offset:2048 sc1\n"
"v_add_f32 v11, s54, v160\n"
"v_rcp_f32 v11, v11\n"
"s_nop 0\n"
"v_mul_f32 v168, v168, v11\n"
"v_mfma_f32_32x32x16_bf16 v[64:79], a[4:7], v[176:179], 0\n"
"v_mul_f32 v169, v169, v11\n"
"v_mul_f32 v170, v170, v11\n"
"v_mul_f32 v171, v171, v11\n"
"v_mul_f32 v172, v172, v11\n"
"v_mul_f32 v173, v173, v11\n"
"v_mul_f32 v174, v174, v11\n"
"v_mul_f32 v175, v175, v11\n"
"v_mfma_f32_32x32x16_bf16 v[48:63], a[0:3], v[176:179], 0\n"
"v_cvt_pk_bf16_f32 v180, v168, v169\n"
"v_cvt_pk_bf16_f32 v181, v170, v171\n"
"v_cvt_pk_bf16_f32 v182, v172, v173\n"
"v_cvt_pk_bf16_f32 v183, v174, v175\n"
"buffer_load_dwordx4 a[76:79], v6, s[16:19], s72 offen offset:3072 sc1\n"
"s_waitcnt vmcnt(12)\n"
"v_mfma_f32_32x32x16_bf16 v[160:175], v[92:95], a[108:111], 0\n"
"s_add_u32 s72, s72, 4096\n"
"buffer_load_dwordx4 a[80:83], v6, s[16:19], s72 offen offset:0 sc1\n"
"v_add_f32 v11, s54, v152\n"
"v_rcp_f32 v11, v11\n"
"s_nop 0\n"
"v_mfma_f32_32x32x16_bf16 v[64:79], a[12:15], v[180:183], v[64:79]\n"
"v_mul_f32 v144, v144, v11\n"
"v_mul_f32 v145, v145, v11\n"
"v_mul_f32 v146, v146, v11\n"
"v_mul_f32 v147, v147, v11\n"
"v_mul_f32 v148, v148, v11\n"
"v_mul_f32 v149, v149, v11\n"
"v_mul_f32 v150, v150, v11\n"
"v_mfma_f32_32x32x16_bf16 v[48:63], a[8:11], v[180:183], v[48:63]\n"
"v_mul_f32 v151, v151, v11\n"
"v_cvt_pk_bf16_f32 v184, v144, v145\n"
"v_cvt_pk_bf16_f32 v185, v146, v147\n"
"v_cvt_pk_bf16_f32 v186, v148, v149\n"
"v_cvt_pk_bf16_f32 v187, v150, v151\n"
"buffer_load_dwordx4 a[84:87], v6, s[16:19], s72 offen offset:1024 sc1\n"
"s_waitcnt vmcnt(12)\n"
"v_mfma_f32_32x32x16_bf16 v[144:159], v[96:99], a[112:115], 0\n"
"buffer_load_dwordx4 a[88:91], v6, s[16:19], s72 offen offset:2048 sc1\n"
"v_add_f32 v11, s54, v160\n"
"v_rcp_f32 v11, v11\n"
"s_nop 0\n"
"v_mul_f32 v168, v168, v11\n"
"v_mfma_f32_32x32x16_bf16 v[64:79], a[20:23], v[184:187], v[64:79]\n"
"v_mul_f32 v169, v169, v11\n"
"v_mul_f32 v170, v170, v11\n"
"v_mul_f32 v171, v171, v11\n"
"v_mul_f32 v172, v172, v11\n"
"v_mul_f32 v173, v173, v11\n"
"v_mul_f32 v174, v174, v11\n"
"v_mul_f32 v175, v175, v11\n"
"v_mfma_f32_32x32x16_bf16 v[48:63], a[16:19], v[184:187], v[48:63]\n"
"v_cvt_pk_bf16_f32 v188, v168, v169\n"
"v_cvt_pk_bf16_f32 v189, v170, v171\n"
"v_cvt_pk_bf16_f32 v190, v172, v173\n"
"v_cvt_pk_bf16_f32 v191, v174, v175\n"
"buffer_load_dwordx4 a[92:95], v6, s[16:19], s72 offen offset:3072 sc1\n"
"s_waitcnt vmcnt(12)\n"
"v_mfma_f32_32x32x16_bf16 v[160:175], v[100:103], a[116:119], 0\n"
"buffer_load_dwordx4 v[208:211], v6, s[16:19], s6 offen sc1\n"
"buffer_load_dwordx4 v[212:215], v6, s[16:19], s7 offen sc1\n"
"v_add_f32 v11, s54, v152\n"
"v_rcp_f32 v11, v11\n"
"s_nop 0\n"
"v_mfma_f32_32x32x16_bf16 v[64:79], a[28:31], v[188:191], v[64:79]\n"
"v_mul_f32 v144, v144, v11\n"
"v_mul_f32 v145, v145, v11\n"
"v_mul_f32 v146, v146, v11\n"
"v_mul_f32 v147, v147, v11\n"
"v_mul_f32 v148, v148, v11\n"
"v_mul_f32 v149, v149, v11\n"
"v_mul_f32 v150, v150, v11\n"
"v_mfma_f32_32x32x16_bf16 v[48:63], a[24:27], v[188:191], v[48:63]\n"
"v_mul_f32 v151, v151, v11\n"
"v_cvt_pk_bf16_f32 v192, v144, v145\n"
"v_cvt_pk_bf16_f32 v193, v146, v147\n"
"v_cvt_pk_bf16_f32 v194, v148, v149\n"
"v_cvt_pk_bf16_f32 v195, v150, v151\n"
"s_waitcnt vmcnt(12)\n"
"v_mfma_f32_32x32x16_bf16 v[144:159], v[104:107], a[120:123], 0\n"
"v_add_f32 v11, s54, v160\n"
"v_rcp_f32 v11, v11\n"
"s_nop 0\n"
"v_mul_f32 v168, v168, v11\n"
"v_mul_f32 v169, v169, v11\n"
"v_mul_f32 v170, v170, v11\n"
"v_mfma_f32_32x32x16_bf16 v[64:79], a[36:39], v[192:195], v[64:79]\n"
"v_mul_f32 v171, v171, v11\n"
"v_mul_f32 v172, v172, v11\n"
"v_mul_f32 v173, v173, v11\n"
"v_mul_f32 v174, v174, v11\n"
"v_mul_f32 v175, v175, v11\n"
"v_cvt_pk_bf16_f32 v196, v168, v169\n"
"v_mfma_f32_32x32x16_bf16 v[48:63], a[32:35], v[192:195], v[48:63]\n"
"v_cvt_pk_bf16_f32 v197, v170, v171\n"
"v_cvt_pk_bf16_f32 v198, v172, v173\n"
"v_cvt_pk_bf16_f32 v199, v174, v175\n"
"s_waitcnt vmcnt(10)\n"
"v_mfma_f32_32x32x16_bf16 v[160:175], v[108:111], a[124:127], 0\n"
"v_add_f32 v11, s54, v152\n"
"v_rcp_f32 v11, v11\n"
"s_nop 0\n"
"v_mul_f32 v144, v144, v11\n"
"v_mul_f32 v145, v145, v11\n"
"v_mul_f32 v146, v146, v11\n"
"v_mfma_f32_32x32x16_bf16 v[64:79], a[44:47], v[196:199], v[64:79]\n"
"v_mul_f32 v147, v147, v11\n"
"v_mul_f32 v148, v148, v11\n"
"v_mul_f32 v149, v149, v11\n"
"v_mul_f32 v150, v150, v11\n"
"v_mul_f32 v151, v151, v11\n"
"v_cvt_pk_bf16_f32 v200, v144, v145\n"
"v_mfma_f32_32x32x16_bf16 v[48:63], a[40:43], v[196:199], v[48:63]\n"
"v_cvt_pk_bf16_f32 v201, v146, v147\n"
"v_cvt_pk_bf16_f32 v202, v148, v149\n"
"v_cvt_pk_bf16_f32 v203, v150, v151\n"
"s_waitcnt vmcnt(9)\n"
"v_mfma_f32_32x32x16_bf16 v[144:159], v[80:83], a[64:67], 0\n"
"v_add_f32 v11, s54, v160\n"
"v_rcp_f32 v11, v11\n"
"s_nop 0\n"
"v_mul_f32 v168, v168, v11\n"
"v_mul_f32 v169, v169, v11\n"
"v_mul_f32 v170, v170, v11\n"
"v_mfma_f32_32x32x16_bf16 v[64:79], a[52:55], v[200:203], v[64:79]\n"
"v_mul_f32 v171, v171, v11\n"
"v_mul_f32 v172, v172, v11\n"
"v_mul_f32 v173, v173, v11\n"
"v_mul_f32 v174, v174, v11\n"
"v_mul_f32 v175, v175, v11\n"
"v_cvt_pk_bf16_f32 v204, v168, v169\n"
"v_mfma_f32_32x32x16_bf16 v[48:63], a[48:51], v[200:203], v[48:63]\n"
"v_cvt_pk_bf16_f32 v205, v170, v171\n"
"v_cvt_pk_bf16_f32 v206, v172, v173\n"
"v_cvt_pk_bf16_f32 v207, v174, v175\n"
"s_waitcnt vmcnt(8)\n"
"v_mfma_f32_32x32x16_bf16 v[160:175], v[84:87], a[68:71], 0\n"
"v_add_f32 v11, s54, v152\n"
"v_rcp_f32 v11, v11\n"
"s_nop 0\n"
"v_mul_f32 v144, v144, v11\n"
"v_mul_f32 v145, v145, v11\n"
"v_mul_f32 v146, v146, v11\n"
"v_mfma_f32_32x32x16_bf16 v[64:79], a[60:63], v[204:207], v[64:79]\n"
"v_mul_f32 v147, v147, v11\n"
"v_mul_f32 v148, v148, v11\n"
"v_mul_f32 v149, v149, v11\n"
"v_mul_f32 v150, v150, v11\n"
"v_mul_f32 v151, v151, v11\n"
"v_cvt_pk_bf16_f32 v176, v144, v145\n"
"v_mfma_f32_32x32x16_bf16 v[48:63], a[56:59], v[204:207], v[48:63]\n"
"v_cvt_pk_bf16_f32 v177, v146, v147\n"
"v_cvt_pk_bf16_f32 v178, v148, v149\n"
"v_cvt_pk_bf16_f32 v179, v150, v151\n"
"s_add_u32 m0, s75, 65536\n"
"s_add_u32 s65, s63, 0\n"
"buffer_load_dwordx4 v6, s[16:19], s65 offen offset:0 sc1 lds\n"
"s_waitcnt vmcnt(8)\n"
"v_mfma_f32_32x32x16_bf16 v[144:159], v[88:91], a[72:75], 0\n"
"v_add_f32 v11, s54, v160\n"
"v_rcp_f32 v11, v11\n"
"s_nop 0\n"
"v_mul_f32 v168, v168, v11\n"
"v_mul_f32 v169, v169, v11\n"
"v_mul_f32 v170, v170, v11\n"
"v_mfma_f32_32x32x16_bf16 v[32:47], a[4:7], v[176:179], 0\n"
"v_mul_f32 v171, v171, v11\n"
"v_mul_f32 v172, v172, v11\n"
"v_mul_f32 v173, v173, v11\n"
"v_mul_f32 v174, v174, v11\n"
"v_mul_f32 v175, v175, v11\n"
"v_cvt_pk_bf16_f32 v180, v168, v169\n"
"v_cvt_pk_bf16_f32 v181, v170, v171\n"
"v_cvt_pk_bf16_f32 v182, v172, v173\n"
"v_cvt_pk_bf16_f32 v183, v174, v175\n"
"buffer_load_dwordx4 v6, s[16:19], s65 offen offset:1024 sc1 lds\n"
"s_waitcnt vmcnt(8)\n"
"v_mfma_f32_32x32x16_bf16 v[160:175], v[92:95], a[76:79], 0\n"
"v_add_f32 v11, s54, v152\n"
"v_rcp_f32 v11, v11\n"
"s_nop 0\n"
"v_mul_f32 v144, v144, v11\n"
"v_mul_f32 v145, v145, v11\n"
"v_mul_f32 v146, v146, v11\n"
"v_mfma_f32_32x32x16_bf16 v[32:47], a[12:15], v[180:183], v[32:47]\n"
"v_mul_f32 v147, v147, v11\n"
"v_mul_f32 v148, v148, v11\n"
"v_mul_f32 v149, v149, v11\n"
"v_mul_f32 v150, v150, v11\n"
"v_mul_f32 v151, v151, v11\n"
"v_cvt_pk_bf16_f32 v184, v144, v145\n"
"v_cvt_pk_bf16_f32 v185, v146, v147\n"
"v_cvt_pk_bf16_f32 v186, v148, v149\n"
"v_cvt_pk_bf16_f32 v187, v150, v151\n"
"ds_write_b128 v234, v[64:67] offset:0\n"
"ds_write_b128 v234, v[68:71] offset:1024\n"
"buffer_load_dwordx4 v6, s[16:19], s65 offen offset:2048 sc1 lds\n"
"s_waitcnt vmcnt(8)\n"
"v_mfma_f32_32x32x16_bf16 v[144:159], v[96:99], a[80:83], 0\n"
"v_add_f32 v11, s54, v160\n"
"v_rcp_f32 v11, v11\n"
"s_nop 0\n"
"v_mul_f32 v168, v168, v11\n"
"v_mul_f32 v169, v169, v11\n"
"v_mul_f32 v170, v170, v11\n"
"v_mfma_f32_32x32x16_bf16 v[32:47], a[20:23], v[184:187], v[32:47]\n"
"v_mul_f32 v171, v171, v11\n"
"v_mul_f32 v172, v172, v11\n"
"v_mul_f32 v173, v173, v11\n"
"v_mul_f32 v174, v174, v11\n"
"v_mul_f32 v175, v175, v11\n"
"v_cvt_pk_bf16_f32 v188, v168, v169\n"
"v_cvt_pk_bf16_f32 v189, v170, v171\n"
"v_cvt_pk_bf16_f32 v190, v172, v173\n"
"v_cvt_pk_bf16_f32 v191, v174, v175\n"
"ds_write_b128 v234, v[72:75] offset:2048\n"
"ds_write_b128 v234, v[76:79] offset:3072\n"
"buffer_load_dwordx4 v6, s[16:19], s65 offen offset:3072 sc1 lds\n"
"s_waitcnt vmcnt(8)\n"
"v_mfma_f32_32x32x16_bf16 v[160:175], v[100:103], a[84:87], 0\n"
"v_add_f32 v11, s54, v152\n"
"v_rcp_f32 v11, v11\n"
"s_nop 0\n"
"v_mul_f32 v144, v144, v11\n"
"v_mul_f32 v145, v145, v11\n"
"v_mul_f32 v146, v146, v11\n"
"v_mfma_f32_32x32x16_bf16 v[32:47], a[28:31], v[188:191], v[32:47]\n"
"v_mul_f32 v147, v147, v11\n"
"v_mul_f32 v148, v148, v11\n"
"v_mul_f32 v149, v149, v11\n"
"v_mul_f32 v150, v150, v11\n"
"v_mul_f32 v151, v151, v11\n"
"v_cvt_pk_bf16_f32 v192, v144, v145\n"
"v_cvt_pk_bf16_f32 v193, v146, v147\n"
"v_cvt_pk_bf16_f32 v194, v148, v149\n"
"v_cvt_pk_bf16_f32 v195, v150, v151\n"
"ds_write_b128 v233, v[48:51] offset:0\n"
"ds_write_b128 v233, v[52:55] offset:1024\n"
"s_add_u32 m0, s75, 69632\n"
"s_add_u32 s65, s4, 0\n"
"buffer_load_dwordx4 v6, s[16:19], s65 offen offset:0 sc1 lds\n"
"s_waitcnt vmcnt(8)\n"
"v_mfma_f32_32x32x16_bf16 v[144:159], v[104:107], a[88:91], 0\n"
"v_add_f32 v11, s54, v160\n"
"v_rcp_f32 v11, v11\n"
"s_nop 0\n"
"v_mul_f32 v168, v168, v11\n"
"v_mul_f32 v169, v169, v11\n"
"v_mul_f32 v170, v170, v11\n"
"v_mfma_f32_32x32x16_bf16 v[32:47], a[36:39], v[192:195], v[32:47]\n"
"v_mul_f32 v171, v171, v11\n"
"v_mul_f32 v172, v172, v11\n"
"v_mul_f32 v173, v173, v11\n"
"v_mul_f32 v174, v174, v11\n"
"v_mul_f32 v175, v175, v11\n"
"v_cvt_pk_bf16_f32 v196, v168, v169\n"
"v_cvt_pk_bf16_f32 v197, v170, v171\n"
"v_cvt_pk_bf16_f32 v198, v172, v173\n"
"v_cvt_pk_bf16_f32 v199, v174, v175\n"
"ds_write_b128 v233, v[56:59] offset:2048\n"
"ds_write_b128 v233, v[60:63] offset:3072\n"
"buffer_load_dwordx4 v6, s[16:19], s65 offen offset:1024 sc1 lds\n"
"s_waitcnt vmcnt(8)\n"
"v_mfma_f32_32x32x16_bf16 v[160:175], v[108:111], a[92:95], 0\n"
"v_add_f32 v11, s54, v152\n"
"v_rcp_f32 v11, v11\n"
"s_nop 0\n"
"v_mul_f32 v144, v144, v11\n"
"v_mul_f32 v145, v145, v11\n"
"v_mul_f32 v146, v146, v11\n"
"v_mfma_f32_32x32x16_bf16 v[32:47], a[44:47], v[196:199], v[32:47]\n"
"v_mul_f32 v147, v147, v11\n"
"v_mul_f32 v148, v148, v11\n"
"v_mul_f32 v149, v149, v11\n"
"v_mul_f32 v150, v150, v11\n"
"v_mul_f32 v151, v151, v11\n"
"v_cvt_pk_bf16_f32 v200, v144, v145\n"
"v_cvt_pk_bf16_f32 v201, v146, v147\n"
"v_cvt_pk_bf16_f32 v202, v148, v149\n"
"v_cvt_pk_bf16_f32 v203, v150, v151\n"
"buffer_load_dwordx4 v6, s[16:19], s65 offen offset:2048 sc1 lds\n"
"s_nop 0\n"
"v_mfma_f32_32x32x16_bf16 v[32:47], a[52:55], v[200:203], v[32:47]\n"
"v_add_f32 v11, s54, v160\n"
"v_rcp_f32 v11, v11\n"
"s_nop 0\n"
"v_mul_f32 v168, v168, v11\n"
"v_mul_f32 v169, v169, v11\n"
"v_mul_f32 v170, v170, v11\n"
"v_mul_f32 v171, v171, v11\n"
"v_mul_f32 v172, v172, v11\n"
"v_mul_f32 v173, v173, v11\n"
"v_mul_f32 v174, v174, v11\n"
"v_mul_f32 v175, v175, v11\n"
"v_cvt_pk_bf16_f32 v204, v168, v169\n"
"v_cvt_pk_bf16_f32 v205, v170, v171\n"
"v_cvt_pk_bf16_f32 v206, v172, v173\n"
"v_cvt_pk_bf16_f32 v207, v174, v175\n"
"buffer_load_dwordx4 v6, s[16:19], s65 offen offset:3072 sc1 lds\n"
"s_nop 0\n"
"v_mfma_f32_32x32x16_bf16 v[32:47], a[60:63], v[204:207], v[32:47]\n"
"s_add_u32 m0, s75, 98304\n"
"s_add_u32 s65, s63, 32768\n"
"buffer_load_dwordx4 v6, s[16:19], s65 offen offset:0 sc1 lds\n"
"v_mfma_f32_32x32x16_bf16 v[16:31], a[0:3], v[176:179], 0\n"
"buffer_load_dwordx4 v6, s[16:19], s65 offen offset:1024 sc1 lds\n"
"v_mfma_f32_32x32x16_bf16 v[16:31], a[8:11], v[180:183], v[16:31]\n"
"v_mfma_f32_32x32x16_bf16 v[16:31], a[16:19], v[184:187], v[16:31]\n"
"s_nop 4\n"
"ds_write_b128 v232, v[32:35] offset:0\n"
"buffer_load_dwordx4 v6, s[16:19], s65 offen offset:2048 sc1 lds\n"
"v_mfma_f32_32x32x16_bf16 v[16:31], a[24:27], v[188:191], v[16:31]\n"
"ds_write_b128 v232, v[36:39] offset:1024\n"
"v_mfma_f32_32x32x16_bf16 v[16:31], a[32:35], v[192:195], v[16:31]\n"
"ds_write_b128 v232, v[40:43] offset:2048\n"
"buffer_load_dwordx4 v6, s[16:19], s65 offen offset:3072 sc1 lds\n"
"v_mfma_f32_32x32x16_bf16 v[16:31], a[40:43], v[196:199], v[16:31]\n"
"ds_write_b128 v232, v[44:47] offset:3072\n"
"v_mfma_f32_32x32x16_bf16 v[16:31], a[48:51], v[200:203], v[16:31]\n"
"v_mfma_f32_32x32x16_bf16 v[16:31], a[56:59], v[204:207], v[16:31]\n"
"s_lshl_b32 s67, s52, 1\n"
"s_and_b32 s68, s45, 1\n"
"s_add_u32 s67, s67, s68\n"
"s_lshl_b32 s68, s66, 15\n"
"s_lshl_b32 s69, s67, 11\n"
"s_add_u32 s68, s68, s69\n"
"s_add_u32 s68, s68, 6291456\n"
"s_lshl_b32 s69, s66, 12\n"
"s_lshl_b32 s70, s67, 8\n"
"s_add_u32 s69, s69, s70\n"
"s_add_u32 s69, s69, 524288\n"
"s_waitcnt vmcnt(12)\n"
"v_pk_add_f32 v[208:209], v[208:209], v[212:213]\n"
"v_pk_add_f32 v[210:211], v[210:211], v[214:215]\n"
"s_lshl_b32 s70, s45, 10\n"
"s_add_u32 s70, s70, 49152\n"
"v_add_u32 v13, s70, v6\n"
"ds_write_b128 v13, v[208:211]\n"
"s_waitcnt lgkmcnt(0)\n"
"s_barrier\n"
"ds_read_b128 v[80:83], v235 offset:0\n"
"ds_read_b128 v[84:87], v235 offset:1024\n"
"ds_read_b128 v[88:91], v235 offset:2048\n"
"ds_read_b128 v[92:95], v235 offset:3072\n"
"ds_read_b128 v[96:99], v235 offset:4096\n"
"ds_read_b128 v[100:103], v235 offset:5120\n"
"ds_read_b128 v[104:107], v235 offset:6144\n"
"ds_read_b128 v[108:111], v235 offset:7168\n"
"ds_read_b128 v[112:115], v235 offset:8192\n"
"ds_read_b128 v[116:119], v235 offset:9216\n"
"ds_read_b128 v[120:123], v235 offset:10240\n"
"ds_read_b128 v[124:127], v235 offset:11264\n"
"v_lshlrev_b32 v11, 2, v1\n"
"v_pk_add_f32 v[144:145], v[16:17], v[216:217]\n"
"v_pk_add_f32 v[146:147], v[18:19], v[218:219]\n"
"v_pk_add_f32 v[148:149], v[20:21], v[220:221]\n"
"v_pk_add_f32 v[150:151], v[22:23], v[222:223]\n"
"v_pk_add_f32 v[152:153], v[24:25], v[224:225]\n"
"v_pk_add_f32 v[154:155], v[26:27], v[226:227]\n"
"v_pk_add_f32 v[156:157], v[28:29], v[228:229]\n"
"v_pk_add_f32 v[158:159], v[30:31], v[230:231]\n"
"s_waitcnt lgkmcnt(11)\n"
"v_pk_add_f32 v[144:145], v[144:145], v[80:81]\n"
"v_pk_add_f32 v[146:147], v[146:147], v[82:83]\n"
"s_waitcnt lgkmcnt(10)\n"
"v_pk_add_f32 v[148:149], v[148:149], v[84:85]\n"
"v_pk_add_f32 v[150:151], v[150:151], v[86:87]\n"
"s_waitcnt lgkmcnt(9)\n"
"v_pk_add_f32 v[152:153], v[152:153], v[88:89]\n"
"v_pk_add_f32 v[154:155], v[154:155], v[90:91]\n"
"s_waitcnt lgkmcnt(8)\n"
"v_pk_add_f32 v[156:157], v[156:157], v[92:93]\n"
"v_pk_add_f32 v[158:159], v[158:159], v[94:95]\n"
"s_waitcnt lgkmcnt(7)\n"
"v_pk_add_f32 v[144:145], v[144:145], v[96:97]\n"
"v_pk_add_f32 v[146:147], v[146:147], v[98:99]\n"
"s_waitcnt lgkmcnt(6)\n"
"v_pk_add_f32 v[148:149], v[148:149], v[100:101]\n"
"v_pk_add_f32 v[150:151], v[150:151], v[102:103]\n"
"s_waitcnt lgkmcnt(5)\n"
"v_pk_add_f32 v[152:153], v[152:153], v[104:105]\n"
"v_pk_add_f32 v[154:155], v[154:155], v[106:107]\n"
"s_waitcnt lgkmcnt(4)\n"
"v_pk_add_f32 v[156:157], v[156:157], v[108:109]\n"
"v_pk_add_f32 v[158:159], v[158:159], v[110:111]\n"
"s_waitcnt lgkmcnt(3)\n"
"v_pk_add_f32 v[144:145], v[144:145], v[112:113]\n"
"v_pk_add_f32 v[146:147], v[146:147], v[114:115]\n"
"s_waitcnt lgkmcnt(2)\n"
"v_pk_add_f32 v[148:149], v[148:149], v[116:117]\n"
"v_pk_add_f32 v[150:151], v[150:151], v[118:119]\n"
"s_waitcnt lgkmcnt(1)\n"
"v_pk_add_f32 v[152:153], v[152:153], v[120:121]\n"
"v_pk_add_f32 v[154:155], v[154:155], v[122:123]\n"
"s_waitcnt lgkmcnt(0)\n"
"v_pk_add_f32 v[156:157], v[156:157], v[124:125]\n"
"v_pk_add_f32 v[158:159], v[158:159], v[126:127]\n"
"v_cvt_pk_bf16_f32 v128, v144, v145\n"
"v_cvt_pk_bf16_f32 v129, v146, v147\n"
"v_cvt_pk_bf16_f32 v130, v148, v149\n"
"v_cvt_pk_bf16_f32 v131, v150, v151\n"
"buffer_store_dwordx4 v[128:131], v6, s[16:19], s68 offen offset:0 sc1\n"
"v_cvt_pk_bf16_f32 v132, v152, v153\n"
"v_cvt_pk_bf16_f32 v133, v154, v155\n"
"v_cvt_pk_bf16_f32 v134, v156, v157\n"
"v_cvt_pk_bf16_f32 v135, v158, v159\n"
"buffer_store_dwordx4 v[132:135], v6, s[16:19], s68 offen offset:1024 sc1\n"
"v_pk_add_f32 v[12:13], v[144:145], v[146:147]\n"
"v_pk_mul_f32 v[14:15], v[144:145], v[144:145]\n"
"v_pk_fma_f32 v[14:15], v[146:147], v[146:147], v[14:15]\n"
"v_pk_add_f32 v[12:13], v[12:13], v[148:149]\n"
"v_pk_fma_f32 v[14:15], v[148:149], v[148:149], v[14:15]\n"
"v_pk_add_f32 v[12:13], v[12:13], v[150:151]\n"
"v_pk_fma_f32 v[14:15], v[150:151], v[150:151], v[14:15]\n"
"v_pk_add_f32 v[12:13], v[12:13], v[152:153]\n"
"v_pk_fma_f32 v[14:15], v[152:153], v[152:153], v[14:15]\n"
"v_pk_add_f32 v[12:13], v[12:13], v[154:155]\n"
"v_pk_fma_f32 v[14:15], v[154:155], v[154:155], v[14:15]\n"
"v_pk_add_f32 v[12:13], v[12:13], v[156:157]\n"
"v_pk_fma_f32 v[14:15], v[156:157], v[156:157], v[14:15]\n"
"v_pk_add_f32 v[12:13], v[12:13], v[158:159]\n"
"v_pk_fma_f32 v[14:15], v[158:159], v[158:159], v[14:15]\n"
"v_add_f32 v12, v12, v13\n"
"v_add_f32 v14, v14, v15\n"
"s_lshl_b32 s68, s66, 4\n"
"s_add_u32 s68, s68, s67\n"
"v_permlane32_swap_b32 v12, v14\n"
"v_add_f32 v12, v12, v14\n"
"buffer_store_dword v12, v11, s[16:19], s69 offen sc1\n"
"s_lshl_b32 s68, s68, 7\n"
"s_add_u32 s68, s68, 196608\n"
"s_barrier\n"
"s_lshr_b32 s69, s51, 1\n"
"s_lshl_b32 s69, s69, 1\n"
"s_add_u32 s69, s69, s52\n"
"s_and_b32 s70, s69, 0\n"
"s_mul_i32 s69, s52, 229376\n"
"s_mul_i32 s72, s45, 57344\n"
"s_add_u32 s69, s69, s72\n"
"s_add_u32 s69, s69, 12582912\n"
"s_and_b32 s72, s45, 1\n"
"s_lshl_b32 s72, s72, 10\n"
"s_add_u32 s69, s69, s72\n"
"v_add_u32 v2, s69, v6\n"
"v_xor_b32 v3, 1024, v2\n"
"s_mov_b32 s8, s68\n"
"v_add_u32 v11, 49152, v6\n"
"ds_read_b128 v[80:83], v11 offset:0\n"
"ds_read_b128 v[84:87], v11 offset:1024\n"
"ds_read_b128 v[88:91], v11 offset:2048\n"
"ds_read_b128 v[92:95], v11 offset:3072\n"
"s_waitcnt lgkmcnt(2)\n"
"v_pk_add_f32 v[80:81], v[80:81], v[84:85]\n"
"s_waitcnt lgkmcnt(0)\n"
"v_pk_add_f32 v[88:89], v[88:89], v[92:93]\n"
"v_pk_add_f32 v[82:83], v[82:83], v[86:87]\n"
"v_pk_add_f32 v[90:91], v[90:91], v[94:95]\n"
"v_pk_add_f32 v[80:81], v[80:81], v[88:89]\n"
"v_pk_add_f32 v[82:83], v[82:83], v[90:91]\n"
"v_cvt_pk_bf16_f32 v96, v80, v80\n"
"v_cvt_pk_bf16_f32 v100, v81, v81\n"
"v_cvt_pk_bf16_f32 v104, v82, v82\n"
"v_cvt_pk_bf16_f32 v108, v83, v83\n"
"v_lshlrev_b32 v97, 16, v96\n"
"v_lshlrev_b32 v101, 16, v100\n"
"v_lshlrev_b32 v105, 16, v104\n"
"v_lshlrev_b32 v109, 16, v108\n"
"v_sub_f32 v97, v80, v97\n"
"v_sub_f32 v101, v81, v101\n"
"v_sub_f32 v105, v82, v105\n"
"v_sub_f32 v109, v83, v109\n"
"v_cvt_pk_bf16_f32 v97, v97, v97\n"
"v_mov_b32 v98, 0\n"
"v_mov_b32 v99, 0\n"
"v_cvt_pk_bf16_f32 v101, v101, v101\n"
"v_mov_b32 v102, 0\n"
"v_mov_b32 v103, 0\n"
"v_cvt_pk_bf16_f32 v105, v105, v105\n"
"v_mov_b32 v106, 0\n"
"v_mov_b32 v107, 0\n"
"v_cvt_pk_bf16_f32 v109, v109, v109\n"
"v_mov_b32 v110, 0\n"
"v_mov_b32 v111, 0\n"
"v_lshlrev_b32 v11, 6, v1\n"
"v_add_u32 v11, 53248, v11\n"
"ds_write_b128 v11, v[96:99] offset:0\n"
"ds_write_b128 v11, v[100:103] offset:16\n"
"ds_write_b128 v11, v[104:107] offset:32\n"
"ds_write_b128 v11, v[108:111] offset:48\n"
"v_and_b32 v14, 31, v1\n"
"v_lshlrev_b32 v14, 4, v14\n"
"v_add_u32 v14, 53248, v14\n"
"v_add_u32 v7, 0x10000, v6\n"
"v_add_u32 v8, 0x20000, v6\n"
"s_lshl_b32 s73, s50, 2\n"
"s_add_u32 s73, s73, s45\n"
"s_lshl_b32 s69, s73, 11\n"
"s_add_u32 s69, s69, 196608\n"
"v_and_b32 v11, 15, v1\n"
"v_lshlrev_b32 v11, 7, v11\n"
"s_lshl_b32 s77, s73, 15\n"
"s_add_u32 s77, s77, 6291456\n"
"v_add_u32 v4, s77, v6\n"
"s_lshl_b32 s67, s5, 12\n"
"s_lshl_b32 s68, s73, 12\n"
"s_add_u32 s68, s68, 524288\n"
"s_mul_i32 s74, s73, 114688\n"
"s_lshr_b32 s77, s49, 2\n"
"s_mul_i32 s77, s77, 28\n"
"s_and_b32 s72, s49, 3\n"
"s_add_u32 s77, s77, s72\n"
"s_lshl_b32 s77, s77, 10\n"
"s_add_u32 s74, s74, s77\n"
"s_add_u32 s74, s74, 14680064\n"
"s_lshl_b32 s6, s73, 4\n"
"s_add_u32 s6, s6, s49\n"
"s_lshl_b32 s6, s6, 7\n"
"s_add_u32 s6, s6, 327680\n"
"s_mul_i32 s77, s66, 114688\n"
"s_mul_i32 s9, s45, 28672\n"
"s_add_u32 s9, s9, 14680064\n"
"s_add_u32 s77, s77, s9\n"
"v_add_u32 v108, s77, v6\n"
"s_xor_b32 s77, s66, 1\n"
"s_mul_i32 s77, s77, 114688\n"
"s_add_u32 s77, s77, s9\n"
"v_add_u32 v109, s77, v6\n"
"s_lshl_b32 s7, s51, 12\n"
"s_lshl_b32 s77, s45, 9\n"
"s_add_u32 s7, s7, s77\n"
"s_add_u32 s7, s7, 327680\n"
"v_bfe_u32 v110, v1, 2, 1\n"
"v_lshlrev_b32 v110, 11, v110\n"
"v_and_b32 v12, 3, v1\n"
"v_lshl_add_u32 v110, v12, 7, v110\n"
"s_xor_b32 s77, s45, 1\n"
"s_mul_i32 s77, s77, 12288\n"
"s_add_u32 s77, s77, 0\n"
"v_add_u32 v116, s77, v6\n"
"s_xor_b32 s77, s45, 2\n"
"s_mul_i32 s77, s77, 12288\n"
"s_add_u32 s77, s77, 4096\n"
"v_add_u32 v117, s77, v6\n"
"s_xor_b32 s77, s45, 3\n"
"s_mul_i32 s77, s77, 12288\n"
"s_add_u32 s77, s77, 8192\n"
"v_add_u32 v118, s77, v6\n"
"s_mul_i32 s77, s45, 12288\n"
"s_add_u32 s77, s77, 0\n"
"v_add_u32 v119, s77, v6\n"
"v_lshlrev_b32 v15, 2, v1\n"
"s_waitcnt vmcnt(0)\n"
"s_mov_b64 exec, 1\n"
"buffer_store_dword v9, off, s[16:19], s8 offset:0 sc1\n"
"s_mov_b64 exec, -1\n"
"s_add_u32 m0, s75, 102400\n"
"s_add_u32 s65, s4, 32768\n"
"buffer_load_dwordx4 v6, s[16:19], s65 offen offset:0 sc1 lds\n"
"buffer_load_dwordx4 v6, s[16:19], s65 offen offset:1024 sc1 lds\n"
"buffer_load_dwordx4 v6, s[16:19], s65 offen offset:2048 sc1 lds\n"
"buffer_load_dwordx4 v6, s[16:19], s65 offen offset:3072 sc1 lds\n"
"s_add_u32 m0, s75, 131072\n"
"s_add_u32 s65, s63, 65536\n"
"buffer_load_dwordx4 v6, s[16:19], s65 offen offset:0 sc1 lds\n"
"buffer_load_dwordx4 v6, s[16:19], s65 offen offset:1024 sc1 lds\n"
"buffer_load_dwordx4 v6, s[16:19], s65 offen offset:2048 sc1 lds\n"
"buffer_load_dwordx4 v6, s[16:19], s65 offen offset:3072 sc1 lds\n"
"s_add_u32 m0, s75, 135168\n"
"s_add_u32 s65, s4, 65536\n"
"buffer_load_dwordx4 v6, s[16:19], s65 offen offset:0 sc1 lds\n"
"buffer_load_dwordx4 v6, s[16:19], s65 offen offset:1024 sc1 lds\n"
"buffer_load_dwordx4 v6, s[16:19], s65 offen offset:2048 sc1 lds\n"
"buffer_load_dwordx4 v6, s[16:19], s65 offen offset:3072 sc1 lds\n"
"s_waitcnt vmcnt(0)\n"
"s_waitcnt lgkmcnt(0)\n"
"s_barrier\n"
"ds_read_b128 v[80:83], v7 offset:0\n"
"ds_read_b128 v[84:87], v7 offset:32768\n"
"ds_read_b128 v[88:91], v8 offset:0\n"
"ds_read_b128 v[92:95], v7 offset:1024\n"
"s_mov_b32 s53, 32768\n"
".LK_h2_8:\n"
"buffer_load_dword v12, v11, s[16:19], s69 offen sc1\n"
"s_waitcnt vmcnt(0)\n"
"v_cmp_ne_u32 vcc, s44, v12\n"
"s_cbranch_vccz .LK_h2x_9\n"
"s_sub_u32 s53, s53, 1\n"
"s_cbranch_scc0 .LK_h2_8\n"
".LK_h2x_9:\n"
"buffer_load_dwordx4 a[0:3], v4, s[16:19], s67 offen offset:0 sc1\n"
"buffer_load_dwordx4 a[4:7], v4, s[16:19], s67 offen offset:1024 sc1\n"
"buffer_load_dwordx4 a[8:11], v4, s[16:19], s67 offen offset:2048 sc1\n"
"buffer_load_dwordx4 a[12:15], v4, s[16:19], s67 offen offset:3072 sc1\n"
"s_add_u32 s67, s67, 4096\n"
"s_and_b32 s67, s67, 0x7fff\n"
"buffer_load_dwordx4 a[16:19], v4, s[16:19], s67 offen offset:0 sc1\n"
"buffer_load_dwordx4 a[20:23], v4, s[16:19], s67 offen offset:1024 sc1\n"
"buffer_load_dwordx4 a[24:27], v4, s[16:19], s67 offen offset:2048 sc1\n"
"buffer_load_dwordx4 a[28:31], v4, s[16:19], s67 offen offset:3072 sc1\n"
"s_add_u32 s67, s67, 4096\n"
"s_and_b32 s67, s67, 0x7fff\n"
"buffer_load_dwordx4 a[32:35], v4, s[16:19], s67 offen offset:0 sc1\n"
"buffer_load_dwordx4 a[36:39], v4, s[16:19], s67 offen offset:1024 sc1\n"
"s_waitcnt lgkmcnt(2)\n"
"s_waitcnt vmcnt(9)\n"
"v_mfma_f32_32x32x16_bf16 v[16:31], v[80:83], a[0:3], 0\n"
"ds_read_b128 v[96:99], v7 offset:33792\n"
"buffer_load_dwordx4 a[40:43], v4, s[16:19], s67 offen offset:2048 sc1\n"
"v_mfma_f32_32x32x16_bf16 v[32:47], v[84:87], a[0:3], 0\n"
"ds_read_b128 v[100:103], v8 offset:1024\n"
"s_waitcnt lgkmcnt(2)\n"
"v_mfma_f32_32x32x16_bf16 v[48:63], v[88:91], a[0:3], 0\n"
"ds_read_b128 v[80:83], v7 offset:2048\n"
"buffer_load_dwordx4 a[44:47], v4, s[16:19], s67 offen offset:3072 sc1\n"
"s_waitcnt vmcnt(10)\n"
"v_mfma_f32_32x32x16_bf16 v[16:31], v[92:95], a[4:7], v[16:31]\n"
"ds_read_b128 v[84:87], v7 offset:34816\n"
"s_waitcnt lgkmcnt(2)\n"
"v_mfma_f32_32x32x16_bf16 v[32:47], v[96:99], a[4:7], v[32:47]\n"
"ds_read_b128 v[88:91], v8 offset:2048\n"
"v_mfma_f32_32x32x16_bf16 v[48:63], v[100:103], a[4:7], v[48:63]\n"
"ds_read_b128 v[92:95], v7 offset:3072\n"
"s_add_u32 s67, s67, 4096\n"
"s_and_b32 s67, s67, 0x7fff\n"
"buffer_load_dwordx4 a[48:51], v4, s[16:19], s67 offen offset:0 sc1\n"
"s_waitcnt lgkmcnt(2)\n"
"s_waitcnt vmcnt(10)\n"
"v_mfma_f32_32x32x16_bf16 v[16:31], v[80:83], a[8:11], v[16:31]\n"
"ds_read_b128 v[96:99], v7 offset:35840\n"
"v_mfma_f32_32x32x16_bf16 v[32:47], v[84:87], a[8:11], v[32:47]\n"
"ds_read_b128 v[100:103], v8 offset:3072\n"
"buffer_load_dwordx4 a[52:55], v4, s[16:19], s67 offen offset:1024 sc1\n"
"s_waitcnt lgkmcnt(2)\n"
"v_mfma_f32_32x32x16_bf16 v[48:63], v[88:91], a[8:11], v[48:63]\n"
"ds_read_b128 v[80:83], v7 offset:4096\n"
"s_waitcnt vmcnt(10)\n"
"v_mfma_f32_32x32x16_bf16 v[16:31], v[92:95], a[12:15], v[16:31]\n"
"ds_read_b128 v[84:87], v7 offset:36864\n"
"s_waitcnt lgkmcnt(2)\n"
"v_mfma_f32_32x32x16_bf16 v[32:47], v[96:99], a[12:15], v[32:47]\n"
"ds_read_b128 v[88:91], v8 offset:4096\n"
"buffer_load_dwordx4 a[56:59], v4, s[16:19], s67 offen offset:2048 sc1\n"
"v_mfma_f32_32x32x16_bf16 v[48:63], v[100:103], a[12:15], v[48:63]\n"
"ds_read_b128 v[92:95], v7 offset:5120\n"
"s_waitcnt lgkmcnt(2)\n"
"s_waitcnt vmcnt(10)\n"
"v_mfma_f32_32x32x16_bf16 v[16:31], v[80:83], a[16:19], v[16:31]\n"
"ds_read_b128 v[96:99], v7 offset:37888\n"
"buffer_load_dwordx4 a[60:63], v4, s[16:19], s67 offen offset:3072 sc1\n"
"v_mfma_f32_32x32x16_bf16 v[32:47], v[84:87], a[16:19], v[32:47]\n"
"ds_read_b128 v[100:103], v8 offset:5120\n"
"s_waitcnt lgkmcnt(2)\n"
"v_mfma_f32_32x32x16_bf16 v[48:63], v[88:91], a[16:19], v[48:63]\n"
"ds_read_b128 v[80:83], v7 offset:6144\n"
"s_waitcnt vmcnt(10)\n"
"v_mfma_f32_32x32x16_bf16 v[16:31], v[92:95], a[20:23], v[16:31]\n"
"ds_read_b128 v[84:87], v7 offset:38912\n"
"s_add_u32 s67, s67, 4096\n"
"s_and_b32 s67, s67, 0x7fff\n"
"buffer_load_dwordx4 a[64:67], v4, s[16:19], s67 offen offset:0 sc1\n"
"s_waitcnt lgkmcnt(2)\n"
"v_mfma_f32_32x32x16_bf16 v[32:47], v[96:99], a[20:23], v[32:47]\n"
"ds_read_b128 v[88:91], v8 offset:6144\n"
"v_mfma_f32_32x32x16_bf16 v[48:63], v[100:103], a[20:23], v[48:63]\n"
"ds_read_b128 v[92:95], v7 offset:7168\n"
"buffer_load_dwordx4 a[68:71], v4, s[16:19], s67 offen offset:1024 sc1\n"
"s_waitcnt lgkmcnt(2)\n"
"s_waitcnt vmcnt(11)\n"
"v_mfma_f32_32x32x16_bf16 v[16:31], v[80:83], a[24:27], v[16:31]\n"
"ds_read_b128 v[96:99], v7 offset:39936\n"
"v_mfma_f32_32x32x16_bf16 v[32:47], v[84:87], a[24:27], v[32:47]\n"
"ds_read_b128 v[100:103], v8 offset:7168\n"
"s_waitcnt lgkmcnt(2)\n"
"v_mfma_f32_32x32x16_bf16 v[48:63], v[88:91], a[24:27], v[48:63]\n"
"ds_read_b128 v[80:83], v7 offset:8192\n"
"buffer_load_dwordx4 a[72:75], v4, s[16:19], s67 offen offset:2048 sc1\n"
"s_waitcnt vmcnt(11)\n"
"v_mfma_f32_32x32x16_bf16 v[16:31], v[92:95], a[28:31], v[16:31]\n"
"ds_read_b128 v[84:87], v7 offset:40960\n"
"s_waitcnt lgkmcnt(2)\n"
"v_mfma_f32_32x32x16_bf16 v[32:47], v[96:99], a[28:31], v[32:47]\n"
"ds_read_b128 v[88:91], v8 offset:8192\n"
"buffer_load_dwordx4 a[76:79], v4, s[16:19], s67 offen offset:3072 sc1\n"
"v_mfma_f32_32x32x16_bf16 v[48:63], v[100:103], a[28:31], v[48:63]\n"
"ds_read_b128 v[92:95], v7 offset:9216\n"
"s_waitcnt lgkmcnt(2)\n"
"s_waitcnt vmcnt(11)\n"
"v_mfma_f32_32x32x16_bf16 v[16:31], v[80:83], a[32:35], v[16:31]\n"
"ds_read_b128 v[96:99], v7 offset:41984\n"
"v_mfma_f32_32x32x16_bf16 v[32:47], v[84:87], a[32:35], v[32:47]\n"
"ds_read_b128 v[100:103], v8 offset:9216\n"
"s_add_u32 s67, s67, 4096\n"
"s_and_b32 s67, s67, 0x7fff\n"
"buffer_load_dwordx4 a[80:83], v4, s[16:19], s67 offen offset:0 sc1\n"
"s_waitcnt lgkmcnt(2)\n"
"v_mfma_f32_32x32x16_bf16 v[48:63], v[88:91], a[32:35], v[48:63]\n"
"ds_read_b128 v[80:83], v7 offset:10240\n"
"s_waitcnt vmcnt(11)\n"
"v_mfma_f32_32x32x16_bf16 v[16:31], v[92:95], a[36:39], v[16:31]\n"
"ds_read_b128 v[84:87], v7 offset:43008\n"
"buffer_load_dwordx4 a[84:87], v4, s[16:19], s67 offen offset:1024 sc1\n"
"s_waitcnt lgkmcnt(2)\n"
"v_mfma_f32_32x32x16_bf16 v[32:47], v[96:99], a[36:39], v[32:47]\n"
"ds_read_b128 v[88:91], v8 offset:10240\n"
"v_mfma_f32_32x32x16_bf16 v[48:63], v[100:103], a[36:39], v[48:63]\n"
"ds_read_b128 v[92:95], v7 offset:11264\n"
"s_waitcnt lgkmcnt(2)\n"
"s_waitcnt vmcnt(11)\n"
"v_mfma_f32_32x32x16_bf16 v[16:31], v[80:83], a[40:43], v[16:31]\n"
"ds_read_b128 v[96:99], v7 offset:44032\n"
"buffer_load_dwordx4 a[88:91], v4, s[16:19], s67 offen offset:2048 sc1\n"
"v_mfma_f32_32x32x16_bf16 v[32:47], v[84:87], a[40:43], v[32:47]\n"
"ds_read_b128 v[100:103], v8 offset:11264\n"
"s_waitcnt lgkmcnt(2)\n"
"v_mfma_f32_32x32x16_bf16 v[48:63], v[88:91], a[40:43], v[48:63]\n"
"ds_read_b128 v[80:83], v7 offset:12288\n"
"buffer_load_dwordx4 a[92:95], v4, s[16:19], s67 offen offset:3072 sc1\n"
"s_waitcnt vmcnt(12)\n"
"v_mfma_f32_32x32x16_bf16 v[16:31], v[92:95], a[44:47], v[16:31]\n"
"ds_read_b128 v[84:87], v7 offset:45056\n"
"s_waitcnt lgkmcnt(2)\n"
"v_mfma_f32_32x32x16_bf16 v[32:47], v[96:99], a[44:47], v[32:47]\n"
"ds_read_b128 v[88:91], v8 offset:12288\n"
"v_mfma_f32_32x32x16_bf16 v[48:63], v[100:103], a[44:47], v[48:63]\n"
"ds_read_b128 v[92:95], v7 offset:13312\n"
"s_add_u32 s67, s67, 4096\n"
"s_and_b32 s67, s67, 0x7fff\n"
"buffer_load_dwordx4 a[96:99], v4, s[16:19], s67 offen offset:0 sc1\n"
"s_waitcnt lgkmcnt(2)\n"
"s_waitcnt vmcnt(12)\n"
"v_mfma_f32_32x32x16_bf16 v[16:31], v[80:83], a[48:51], v[16:31]\n"
"ds_read_b128 v[96:99], v7 offset:46080\n"
"v_mfma_f32_32x32x16_bf16 v[32:47], v[84:87], a[48:51], v[32:47]\n"
"ds_read_b128 v[100:103], v8 offset:13312\n"
"buffer_load_dwordx4 a[100:103], v4, s[16:19], s67 offen offset:1024 sc1\n"
"s_waitcnt lgkmcnt(2)\n"
"v_mfma_f32_32x32x16_bf16 v[48:63], v[88:91], a[48:51], v[48:63]\n"
"ds_read_b128 v[80:83], v7 offset:14336\n"
"s_waitcnt vmcnt(12)\n"
"v_mfma_f32_32x32x16_bf16 v[16:31], v[92:95], a[52:55], v[16:31]\n"
"ds_read_b128 v[84:87], v7 offset:47104\n"
"s_waitcnt lgkmcnt(2)\n"
"v_mfma_f32_32x32x16_bf16 v[32:47], v[96:99], a[52:55], v[32:47]\n"
"ds_read_b128 v[88:91], v8 offset:14336\n"
"buffer_load_dwordx4 a[104:107], v4, s[16:19], s67 offen offset:2048 sc1\n"
"v_mfma_f32_32x32x16_bf16 v[48:63], v[100:103], a[52:55], v[48:63]\n"
"ds_read_b128 v[92:95], v7 offset:15360\n"
"s_waitcnt lgkmcnt(2)\n"
"s_waitcnt vmcnt(12)\n"
"v_mfma_f32_32x32x16_bf16 v[16:31], v[80:83], a[56:59], v[16:31]\n"
"ds_read_b128 v[96:99], v7 offset:48128\n"
"buffer_load_dwordx4 a[108:111], v4, s[16:19], s67 offen offset:3072 sc1\n"
"v_mfma_f32_32x32x16_bf16 v[32:47], v[84:87], a[56:59], v[32:47]\n"
"ds_read_b128 v[100:103], v8 offset:15360\n"
"s_waitcnt lgkmcnt(2)\n"
"v_mfma_f32_32x32x16_bf16 v[48:63], v[88:91], a[56:59], v[48:63]\n"
"ds_read_b128 v[80:83], v7 offset:16384\n"
"buffer_load_dword v120, v15, s[16:19], s68 offen offset:0 sc1\n"
"s_waitcnt vmcnt(13)\n"
"v_mfma_f32_32x32x16_bf16 v[16:31], v[92:95], a[60:63], v[16:31]\n"
"ds_read_b128 v[84:87], v7 offset:49152\n"
"s_add_u32 s67, s67, 4096\n"
"s_and_b32 s67, s67, 0x7fff\n"
"buffer_load_dwordx4 a[112:115], v4, s[16:19], s67 offen offset:0 sc1\n"
"buffer_load_dword v121, v15, s[16:19], s68 offen offset:256 sc1\n"
"s_waitcnt lgkmcnt(2)\n"
"v_mfma_f32_32x32x16_bf16 v[32:47], v[96:99], a[60:63], v[32:47]\n"
"ds_read_b128 v[88:91], v8 offset:16384\n"
"buffer_load_dword v122, v15, s[16:19], s68 offen offset:512 sc1\n"
"v_mfma_f32_32x32x16_bf16 v[48:63], v[100:103], a[60:63], v[48:63]\n"
"ds_read_b128 v[92:95], v7 offset:17408\n"
"buffer_load_dwordx4 a[116:119], v4, s[16:19], s67 offen offset:1024 sc1\n"
"buffer_load_dword v123, v15, s[16:19], s68 offen offset:768 sc1\n"
"s_waitcnt lgkmcnt(2)\n"
"s_waitcnt vmcnt(17)\n"
"v_mfma_f32_32x32x16_bf16 v[16:31], v[80:83], a[64:67], v[16:31]\n"
"ds_read_b128 v[96:99], v7 offset:50176\n"
"buffer_load_dword v124, v15, s[16:19], s68 offen offset:1024 sc1\n"
"v_mfma_f32_32x32x16_bf16 v[32:47], v[84:87], a[64:67], v[32:47]\n"
"ds_read_b128 v[100:103], v8 offset:17408\n"
"buffer_load_dword v125, v15, s[16:19], s68 offen offset:1280 sc1\n"
"s_waitcnt lgkmcnt(2)\n"
"v_mfma_f32_32x32x16_bf16 v[48:63], v[88:91], a[64:67], v[48:63]\n"
"ds_read_b128 v[80:83], v7 offset:18432\n"
"buffer_load_dwordx4 a[120:123], v4, s[16:19], s67 offen offset:2048 sc1\n"
"buffer_load_dword v126, v15, s[16:19], s68 offen offset:1536 sc1\n"
"s_waitcnt vmcnt(20)\n"
"v_mfma_f32_32x32x16_bf16 v[16:31], v[92:95], a[68:71], v[16:31]\n"
"ds_read_b128 v[84:87], v7 offset:51200\n"
"buffer_load_dword v127, v15, s[16:19], s68 offen offset:1792 sc1\n"
"s_waitcnt lgkmcnt(2)\n"
"v_mfma_f32_32x32x16_bf16 v[32:47], v[96:99], a[68:71], v[32:47]\n"
"ds_read_b128 v[88:91], v8 offset:18432\n"
"buffer_load_dwordx4 a[124:127], v4, s[16:19], s67 offen offset:3072 sc1\n"
"buffer_load_dword v128, v15, s[16:19], s68 offen offset:2048 sc1\n"
"v_mfma_f32_32x32x16_bf16 v[48:63], v[100:103], a[68:71], v[48:63]\n"
"ds_read_b128 v[92:95], v7 offset:19456\n"
"buffer_load_dword v129, v15, s[16:19], s68 offen offset:2304 sc1\n"
"s_waitcnt lgkmcnt(2)\n"
"s_waitcnt vmcnt(23)\n"
"v_mfma_f32_32x32x16_bf16 v[16:31], v[80:83], a[72:75], v[16:31]\n"
"ds_read_b128 v[96:99], v7 offset:52224\n"
"buffer_load_dword v130, v15, s[16:19], s68 offen offset:2560 sc1\n"
"v_mfma_f32_32x32x16_bf16 v[32:47], v[84:87], a[72:75], v[32:47]\n"
"ds_read_b128 v[100:103], v8 offset:19456\n"
"buffer_load_dword v131, v15, s[16:19], s68 offen offset:2816 sc1\n"
"s_waitcnt lgkmcnt(2)\n"
"v_mfma_f32_32x32x16_bf16 v[48:63], v[88:91], a[72:75], v[48:63]\n"
"ds_read_b128 v[80:83], v7 offset:20480\n"
"buffer_load_dword v132, v15, s[16:19], s68 offen offset:3072 sc1\n"
"s_waitcnt vmcnt(25)\n"
"v_mfma_f32_32x32x16_bf16 v[16:31], v[92:95], a[76:79], v[16:31]\n"
"ds_read_b128 v[84:87], v7 offset:53248\n"
"buffer_load_dword v133, v15, s[16:19], s68 offen offset:3328 sc1\n"
"s_waitcnt lgkmcnt(2)\n"
"v_mfma_f32_32x32x16_bf16 v[32:47], v[96:99], a[76:79], v[32:47]\n"
"ds_read_b128 v[88:91], v8 offset:20480\n"
"buffer_load_dword v134, v15, s[16:19], s68 offen offset:3584 sc1\n"
"v_mfma_f32_32x32x16_bf16 v[48:63], v[100:103], a[76:79], v[48:63]\n"
"ds_read_b128 v[92:95], v7 offset:21504\n"
"buffer_load_dword v135, v15, s[16:19], s68 offen offset:3840 sc1\n"
"s_waitcnt lgkmcnt(2)\n"
"s_waitcnt vmcnt(27)\n"
"v_mfma_f32_32x32x16_bf16 v[16:31], v[80:83], a[80:83], v[16:31]\n"
"ds_read_b128 v[96:99], v7 offset:54272\n"
"v_mfma_f32_32x32x16_bf16 v[32:47], v[84:87], a[80:83], v[32:47]\n"
"ds_read_b128 v[100:103], v8 offset:21504\n"
"s_waitcnt lgkmcnt(2)\n"
"v_mfma_f32_32x32x16_bf16 v[48:63], v[88:91], a[80:83], v[48:63]\n"
"ds_read_b128 v[80:83], v7 offset:22528\n"
"s_add_u32 m0, s75, 0\n"
"s_add_u32 s65, s63, 98304\n"
"buffer_load_dwordx4 v6, s[16:19], s65 offen offset:0 sc1 lds\n"
"s_waitcnt vmcnt(27)\n"
"v_mfma_f32_32x32x16_bf16 v[16:31], v[92:95], a[84:87], v[16:31]\n"
"ds_read_b128 v[84:87], v7 offset:55296\n"
"buffer_load_dwordx4 v6, s[16:19], s65 offen offset:1024 sc1 lds\n"
"s_waitcnt lgkmcnt(2)\n"
"v_mfma_f32_32x32x16_bf16 v[32:47], v[96:99], a[84:87], v[32:47]\n"
"ds_read_b128 v[88:91], v8 offset:22528\n"
"v_mfma_f32_32x32x16_bf16 v[48:63], v[100:103], a[84:87], v[48:63]\n"
"ds_read_b128 v[92:95], v7 offset:23552\n"
"buffer_load_dwordx4 v6, s[16:19], s65 offen offset:2048 sc1 lds\n"
"s_waitcnt lgkmcnt(2)\n"
"s_waitcnt vmcnt(28)\n"
"v_mfma_f32_32x32x16_bf16 v[16:31], v[80:83], a[88:91], v[16:31]\n"
"ds_read_b128 v[96:99], v7 offset:56320\n"
"v_mfma_f32_32x32x16_bf16 v[32:47], v[84:87], a[88:91], v[32:47]\n"
"ds_read_b128 v[100:103], v8 offset:23552\n"
"buffer_load_dwordx4 v6, s[16:19], s65 offen offset:3072 sc1 lds\n"
"s_waitcnt lgkmcnt(2)\n"
"v_mfma_f32_32x32x16_bf16 v[48:63], v[88:91], a[88:91], v[48:63]\n"
"ds_read_b128 v[80:83], v7 offset:24576\n"
"s_waitcnt vmcnt(28)\n"
"v_mfma_f32_32x32x16_bf16 v[16:31], v[92:95], a[92:95], v[16:31]\n"
"ds_read_b128 v[84:87], v7 offset:57344\n"
"s_add_u32 m0, s75, 4096\n"
"s_add_u32 s65, s4, 98304\n"
"buffer_load_dwordx4 v6, s[16:19], s65 offen offset:0 sc1 lds\n"
"s_waitcnt lgkmcnt(2)\n"
"v_mfma_f32_32x32x16_bf16 v[32:47], v[96:99], a[92:95], v[32:47]\n"
"ds_read_b128 v[88:91], v8 offset:24576\n"
"v_mfma_f32_32x32x16_bf16 v[48:63], v[100:103], a[92:95], v[48:63]\n"
"ds_read_b128 v[92:95], v7 offset:25600\n"
"buffer_load_dwordx4 v6, s[16:19], s65 offen offset:1024 sc1 lds\n"
"s_waitcnt lgkmcnt(2)\n"
"s_waitcnt vmcnt(29)\n"
"v_mfma_f32_32x32x16_bf16 v[16:31], v[80:83], a[96:99], v[16:31]\n"
"ds_read_b128 v[96:99], v7 offset:58368\n"
"v_mfma_f32_32x32x16_bf16 v[32:47], v[84:87], a[96:99], v[32:47]\n"
"ds_read_b128 v[100:103], v8 offset:25600\n"
"buffer_load_dwordx4 v6, s[16:19], s65 offen offset:2048 sc1 lds\n"
"s_waitcnt lgkmcnt(2)\n"
"v_mfma_f32_32x32x16_bf16 v[48:63], v[88:91], a[96:99], v[48:63]\n"
"ds_read_b128 v[80:83], v7 offset:26624\n"
"s_waitcnt vmcnt(14)\n"
"v_add_f32 v120, v120, v128\n"
"s_waitcnt vmcnt(13)\n"
"v_add_f32 v121, v121, v129\n"
"s_waitcnt vmcnt(12)\n"
"v_add_f32 v122, v122, v130\n"
"s_waitcnt vmcnt(11)\n"
"v_add_f32 v123, v123, v131\n"
"s_waitcnt vmcnt(10)\n"
"v_add_f32 v124, v124, v132\n"
"s_waitcnt vmcnt(9)\n"
"v_add_f32 v125, v125, v133\n"
"v_mfma_f32_32x32x16_bf16 v[16:31], v[92:95], a[100:103], v[16:31]\n"
"ds_read_b128 v[84:87], v7 offset:59392\n"
"buffer_load_dwordx4 v6, s[16:19], s65 offen offset:3072 sc1 lds\n"
"s_waitcnt vmcnt(9)\n"
"v_add_f32 v126, v126, v134\n"
"s_waitcnt vmcnt(8)\n"
"v_add_f32 v127, v127, v135\n"
"v_add_f32 v120, v120, v124\n"
"v_add_f32 v121, v121, v125\n"
"s_waitcnt lgkmcnt(2)\n"
"v_mfma_f32_32x32x16_bf16 v[32:47], v[96:99], a[100:103], v[32:47]\n"
"ds_read_b128 v[88:91], v8 offset:26624\n"
"v_add_f32 v122, v122, v126\n"
"v_add_f32 v123, v123, v127\n"
"v_add_f32 v120, v120, v122\n"
"v_add_f32 v121, v121, v123\n"
"v_add_f32 v120, v120, v121\n"
"v_mov_b32 v121, v120\n"
"v_mfma_f32_32x32x16_bf16 v[48:63], v[100:103], a[100:103], v[48:63]\n"
"ds_read_b128 v[92:95], v7 offset:27648\n"
"s_mov_b32 s71, 16384\n"
"buffer_load_dwordx4 a[192:195], v2, s[16:19], s71 offen sc1\n"
"s_nop 1\n"
"v_permlane32_swap_b32 v120, v121\n"
"s_waitcnt lgkmcnt(2)\n"
"v_mfma_f32_32x32x16_bf16 v[16:31], v[80:83], a[104:107], v[16:31]\n"
"ds_read_b128 v[96:99], v7 offset:60416\n"
"v_mul_f32 v11, 0xbb000000, v120\n"
"v_mul_f32 v121, 0x3b000000, v121\n"
"v_fma_f32 v13, -v11, v11, v121\n"
"v_max_f32 v13, 0, v13\n"
"v_add_f32 v13, 0x3727c5ac, v13\n"
"v_mfma_f32_32x32x16_bf16 v[32:47], v[84:87], a[104:107], v[32:47]\n"
"ds_read_b128 v[100:103], v8 offset:27648\n"
"buffer_load_dwordx4 a[196:199], v3, s[16:19], s71 offen sc1\n"
"v_rsq_f32 v12, v13\n"
"v_cvt_pk_bf16_f32 v120, v11, v11\n"
"v_mul_f32 v12, 0xbfb8aa3b, v12\n"
"s_waitcnt lgkmcnt(2)\n"
"v_mfma_f32_32x32x16_bf16 v[48:63], v[88:91], a[104:107], v[48:63]\n"
"ds_read_b128 v[80:83], v7 offset:28672\n"
"v_lshlrev_b32 v120, 16, v120\n"
"v_sub_f32 v121, v11, v120\n"
"v_cvt_pk_bf16_f32 v120, v120, v121\n"
"v_mov_b32 v106, 0\n"
"v_mov_b32 v107, 0\n"
"v_cndmask_b32 v104, v120, v106, s[60:61]\n"
"v_mfma_f32_32x32x16_bf16 v[16:31], v[92:95], a[108:111], v[16:31]\n"
"ds_read_b128 v[84:87], v7 offset:61440\n"
"s_mov_b32 s71, 18432\n"
"buffer_load_dwordx4 a[200:203], v2, s[16:19], s71 offen sc1\n"
"v_mov_b32 v105, v104\n"
"s_waitcnt lgkmcnt(2)\n"
"v_mfma_f32_32x32x16_bf16 v[32:47], v[96:99], a[108:111], v[32:47]\n"
"ds_read_b128 v[88:91], v8 offset:28672\n"
"buffer_load_dwordx4 a[204:207], v3, s[16:19], s71 offen sc1\n"
"v_mfma_f32_32x32x16_bf16 v[48:63], v[100:103], a[108:111], v[48:63]\n"
"ds_read_b128 v[92:95], v7 offset:29696\n"
"s_waitcnt lgkmcnt(2)\n"
"v_mfma_f32_32x32x16_bf16 v[16:31], v[80:83], a[112:115], v[16:31]\n"
"ds_read_b128 v[96:99], v7 offset:62464\n"
"s_mov_b32 s71, 20480\n"
"buffer_load_dwordx4 a[208:211], v2, s[16:19], s71 offen sc1\n"
"v_mfma_f32_32x32x16_bf16 v[32:47], v[84:87], a[112:115], v[32:47]\n"
"ds_read_b128 v[100:103], v8 offset:29696\n"
"s_waitcnt lgkmcnt(2)\n"
"v_mfma_f32_32x32x16_bf16 v[48:63], v[88:91], a[112:115], v[48:63]\n"
"ds_read_b128 v[80:83], v7 offset:30720\n"
"buffer_load_dwordx4 a[212:215], v3, s[16:19], s71 offen sc1\n"
"v_mfma_f32_32x32x16_bf16 v[16:31], v[92:95], a[116:119], v[16:31]\n"
"ds_read_b128 v[84:87], v7 offset:63488\n"
"s_waitcnt lgkmcnt(2)\n"
"v_mfma_f32_32x32x16_bf16 v[32:47], v[96:99], a[116:119], v[32:47]\n"
"ds_read_b128 v[88:91], v8 offset:30720\n"
"s_mov_b32 s71, 22528\n"
"buffer_load_dwordx4 a[216:219], v2, s[16:19], s71 offen sc1\n"
"v_mfma_f32_32x32x16_bf16 v[48:63], v[100:103], a[116:119], v[48:63]\n"
"ds_read_b128 v[92:95], v7 offset:31744\n"
"s_waitcnt lgkmcnt(2)\n"
"v_mfma_f32_32x32x16_bf16 v[16:31], v[80:83], a[120:123], v[16:31]\n"
"ds_read_b128 v[96:99], v7 offset:64512\n"
"buffer_load_dwordx4 a[220:223], v3, s[16:19], s71 offen sc1\n"
"v_mfma_f32_32x32x16_bf16 v[32:47], v[84:87], a[120:123], v[32:47]\n"
"ds_read_b128 v[100:103], v8 offset:31744\n"
"s_waitcnt lgkmcnt(2)\n"
"v_mfma_f32_32x32x16_bf16 v[48:63], v[88:91], a[120:123], v[48:63]\n"
"ds_read_b128 v[80:83], v14 offset:0\n"
"s_mov_b32 s71, 24576\n"
"buffer_load_dwordx4 a[224:227], v2, s[16:19], s71 offen sc1\n"
"v_mfma_f32_32x32x16_bf16 v[16:31], v[92:95], a[124:127], v[16:31]\n"
"ds_read_b128 v[84:87], v14 offset:512\n"
"s_waitcnt lgkmcnt(2)\n"
"v_mfma_f32_32x32x16_bf16 v[32:47], v[96:99], a[124:127], v[32:47]\n"
"ds_read_b128 v[88:91], v14 offset:1024\n"
"buffer_load_dwordx4 a[228:231], v3, s[16:19], s71 offen sc1\n"
"v_mfma_f32_32x32x16_bf16 v[48:63], v[100:103], a[124:127], v[48:63]\n"
"s_waitcnt vmcnt(10)\n"
"s_barrier\n"
"ds_read_b128 v[92:95], v6 offset:0\n"
"s_waitcnt lgkmcnt(2)\n"
"v_mfma_f32_32x32x16_bf16 v[16:31], v[80:83], v[104:107], v[16:31]\n"
"ds_read_b128 v[96:99], v6 offset:1024\n"
"s_barrier\n"
"s_mov_b32 s71, 26624\n"
"buffer_load_dwordx4 a[232:235], v2, s[16:19], s71 offen sc1\n"
"v_mfma_f32_32x32x16_bf16 v[32:47], v[84:87], v[104:107], v[32:47]\n"
"ds_read_b128 v[100:103], v6 offset:2048\n"
"s_waitcnt lgkmcnt(2)\n"
"v_mfma_f32_32x32x16_bf16 v[48:63], v[88:91], v[104:107], v[48:63]\n"
"ds_read_b128 v[80:83], v6 offset:3072\n"
"s_add_u32 m0, s75, 65536\n"
"s_add_u32 s65, s63, 131072\n"
"buffer_load_dwordx4 v6, s[16:19], s65 offen offset:0 sc1 lds\n"
"v_mul_f32 v120, v12, v16\n"
"v_mul_f32 v121, v12, v17\n"
"v_mul_f32 v122, v12, v18\n"
"v_mfma_f32_32x32x16_bf16 v[64:79], v[92:95], a[0:3], 0\n"
"ds_read_b128 v[84:87], v6 offset:4096\n"
"v_mul_f32 v123, v12, v19\n"
"v_exp_f32 v120, v120\n"
"v_mul_f32 v128, v16, v24\n"
"v_exp_f32 v121, v121\n"
"s_waitcnt lgkmcnt(2)\n"
"v_mfma_f32_32x32x16_bf16 v[64:79], v[96:99], a[4:7], v[64:79]\n"
"ds_read_b128 v[88:91], v6 offset:5120\n"
"buffer_load_dwordx4 v6, s[16:19], s65 offen offset:1024 sc1 lds\n"
"v_mul_f32 v129, v17, v25\n"
"v_exp_f32 v122, v122\n"
"v_mul_f32 v130, v18, v26\n"
"v_mfma_f32_32x32x16_bf16 v[64:79], v[100:103], a[8:11], v[64:79]\n"
"ds_read_b128 v[92:95], v6 offset:6144\n"
"buffer_load_dwordx4 v6, s[16:19], s65 offen offset:2048 sc1 lds\n"
"v_exp_f32 v123, v123\n"
"v_mul_f32 v131, v19, v27\n"
"v_fma_f32 v120, v120, v13, v13\n"
"s_waitcnt lgkmcnt(2)\n"
"v_mfma_f32_32x32x16_bf16 v[64:79], v[80:83], a[12:15], v[64:79]\n"
"ds_read_b128 v[96:99], v6 offset:7168\n"
"v_fma_f32 v121, v121, v13, v13\n"
"v_fma_f32 v122, v122, v13, v13\n"
"v_fma_f32 v123, v123, v13, v13\n"
"v_rcp_f32 v120, v120\n"
"v_mfma_f32_32x32x16_bf16 v[64:79], v[84:87], a[16:19], v[64:79]\n"
"ds_read_b128 v[100:103], v6 offset:8192\n"
"buffer_load_dwordx4 v6, s[16:19], s65 offen offset:3072 sc1 lds\n"
"v_rcp_f32 v121, v121\n"
"v_rcp_f32 v122, v122\n"
"s_waitcnt lgkmcnt(2)\n"
"v_mfma_f32_32x32x16_bf16 v[64:79], v[88:91], a[20:23], v[64:79]\n"
"ds_read_b128 v[80:83], v6 offset:9216\n"
"v_rcp_f32 v123, v123\n"
"v_mul_f32 v120, v120, v128\n"
"v_mul_f32 v121, v121, v129\n"
"v_mul_f32 v122, v122, v130\n"
"v_mul_f32 v123, v123, v131\n"
"v_mfma_f32_32x32x16_bf16 v[64:79], v[92:95], a[24:27], v[64:79]\n"
"ds_read_b128 v[84:87], v6 offset:10240\n"
"s_add_u32 m0, s75, 69632\n"
"s_add_u32 s65, s4, 131072\n"
"buffer_load_dwordx4 v6, s[16:19], s65 offen offset:0 sc1 lds\n"
"v_cvt_pk_bf16_f32 v136, v120, v121\n"
"s_waitcnt lgkmcnt(2)\n"
"v_mfma_f32_32x32x16_bf16 v[64:79], v[96:99], a[28:31], v[64:79]\n"
"ds_read_b128 v[88:91], v6 offset:11264\n"
"v_cvt_pk_bf16_f32 v137, v122, v123\n"
"v_mul_f32 v124, v12, v20\n"
"v_mul_f32 v125, v12, v21\n"
"v_mul_f32 v126, v12, v22\n"
"v_mul_f32 v127, v12, v23\n"
"v_exp_f32 v124, v124\n"
"v_mfma_f32_32x32x16_bf16 v[64:79], v[100:103], a[32:35], v[64:79]\n"
"ds_read_b128 v[92:95], v6 offset:12288\n"
"buffer_load_dwordx4 v6, s[16:19], s65 offen offset:1024 sc1 lds\n"
"v_mul_f32 v132, v20, v28\n"
"v_exp_f32 v125, v125\n"
"s_waitcnt lgkmcnt(2)\n"
"v_mfma_f32_32x32x16_bf16 v[64:79], v[80:83], a[36:39], v[64:79]\n"
"ds_read_b128 v[96:99], v6 offset:13312\n"
"v_mul_f32 v133, v21, v29\n"
"v_exp_f32 v126, v126\n"
"v_mul_f32 v134, v22, v30\n"
"v_exp_f32 v127, v127\n"
"v_mfma_f32_32x32x16_bf16 v[64:79], v[84:87], a[40:43], v[64:79]\n"
"ds_read_b128 v[100:103], v6 offset:14336\n"
"buffer_load_dwordx4 v6, s[16:19], s65 offen offset:2048 sc1 lds\n"
"v_mul_f32 v135, v23, v31\n"
"v_fma_f32 v124, v124, v13, v13\n"
"v_fma_f32 v125, v125, v13, v13\n"
"v_fma_f32 v126, v126, v13, v13\n"
"s_waitcnt lgkmcnt(2)\n"
"v_mfma_f32_32x32x16_bf16 v[64:79], v[88:91], a[44:47], v[64:79]\n"
"ds_read_b128 v[80:83], v6 offset:15360\n"
"v_fma_f32 v127, v127, v13, v13\n"
"v_rcp_f32 v124, v124\n"
"v_rcp_f32 v125, v125\n"
"v_mfma_f32_32x32x16_bf16 v[64:79], v[92:95], a[48:51], v[64:79]\n"
"ds_read_b128 v[84:87], v6 offset:16384\n"
"buffer_load_dwordx4 v6, s[16:19], s65 offen offset:3072 sc1 lds\n"
"v_rcp_f32 v126, v126\n"
"v_rcp_f32 v127, v127\n"
"s_waitcnt lgkmcnt(2)\n"
"v_mfma_f32_32x32x16_bf16 v[64:79], v[96:99], a[52:55], v[64:79]\n"
"ds_read_b128 v[88:91], v6 offset:17408\n"
"v_mul_f32 v124, v124, v132\n"
"v_mul_f32 v125, v125, v133\n"
"v_mul_f32 v126, v126, v134\n"
"v_mul_f32 v127, v127, v135\n"
"v_cvt_pk_bf16_f32 v138, v124, v125\n"
"v_cvt_pk_bf16_f32 v139, v126, v127\n"
"v_mfma_f32_32x32x16_bf16 v[64:79], v[100:103], a[56:59], v[64:79]\n"
"ds_read_b128 v[92:95], v6 offset:18432\n"
"buffer_load_dwordx4 a[236:239], v3, s[16:19], s71 offen sc1\n"
"v_mul_f32 v120, v12, v32\n"
"v_mul_f32 v121, v12, v33\n"
"v_mul_f32 v122, v12, v34\n"
"v_mul_f32 v123, v12, v35\n"
"s_waitcnt lgkmcnt(2)\n"
"v_mfma_f32_32x32x16_bf16 v[64:79], v[80:83], a[60:63], v[64:79]\n"
"ds_read_b128 v[96:99], v6 offset:19456\n"
"v_exp_f32 v120, v120\n"
"v_mul_f32 v128, v32, v40\n"
"v_exp_f32 v121, v121\n"
"v_mul_f32 v129, v33, v41\n"
"v_mfma_f32_32x32x16_bf16 v[64:79], v[84:87], a[64:67], v[64:79]\n"
"ds_read_b128 v[100:103], v6 offset:20480\n"
"s_mov_b32 s71, 28672\n"
"buffer_load_dwordx4 a[240:243], v2, s[16:19], s71 offen sc1\n"
"v_exp_f32 v122, v122\n"
"v_mul_f32 v130, v34, v42\n"
"s_waitcnt lgkmcnt(2)\n"
"v_mfma_f32_32x32x16_bf16 v[64:79], v[88:91], a[68:71], v[64:79]\n"
"ds_read_b128 v[80:83], v6 offset:21504\n"
"v_exp_f32 v123, v123\n"
"v_mul_f32 v131, v35, v43\n"
"v_fma_f32 v120, v120, v13, v13\n"
"v_fma_f32 v121, v121, v13, v13\n"
"v_fma_f32 v122, v122, v13, v13\n"
"v_mfma_f32_32x32x16_bf16 v[64:79], v[92:95], a[72:75], v[64:79]\n"
"ds_read_b128 v[84:87], v6 offset:22528\n"
"buffer_load_dwordx4 a[244:247], v3, s[16:19], s71 offen sc1\n"
"v_fma_f32 v123, v123, v13, v13\n"
"v_rcp_f32 v120, v120\n"
"s_waitcnt lgkmcnt(2)\n"
"v_mfma_f32_32x32x16_bf16 v[64:79], v[96:99], a[76:79], v[64:79]\n"
"ds_read_b128 v[88:91], v6 offset:23552\n"
"v_rcp_f32 v121, v121\n"
"v_rcp_f32 v122, v122\n"
"v_rcp_f32 v123, v123\n"
"v_mfma_f32_32x32x16_bf16 v[64:79], v[100:103], a[80:83], v[64:79]\n"
"ds_read_b128 v[92:95], v6 offset:24576\n"
"s_mov_b32 s71, 30720\n"
"buffer_load_dwordx4 a[248:251], v2, s[16:19], s71 offen sc1\n"
"v_mul_f32 v120, v120, v128\n"
"v_mul_f32 v121, v121, v129\n"
"v_mul_f32 v122, v122, v130\n"
"s_waitcnt lgkmcnt(2)\n"
"v_mfma_f32_32x32x16_bf16 v[64:79], v[80:83], a[84:87], v[64:79]\n"
"ds_read_b128 v[96:99], v6 offset:25600\n"
"s_add_u32 m0, s75, 98304\n"
"s_add_u32 s65, s63, 163840\n"
"buffer_load_dwordx4 v6, s[16:19], s65 offen offset:0 sc1 lds\n"
"v_mul_f32 v123, v123, v131\n"
"v_cvt_pk_bf16_f32 v140, v120, v121\n"
"v_mfma_f32_32x32x16_bf16 v[64:79], v[84:87], a[88:91], v[64:79]\n"
"ds_read_b128 v[100:103], v6 offset:26624\n"
"v_cvt_pk_bf16_f32 v141, v122, v123\n"
"v_mul_f32 v124, v12, v36\n"
"v_mul_f32 v125, v12, v37\n"
"v_mul_f32 v126, v12, v38\n"
"v_mul_f32 v127, v12, v39\n"
"s_waitcnt lgkmcnt(2)\n"
"v_mfma_f32_32x32x16_bf16 v[64:79], v[88:91], a[92:95], v[64:79]\n"
"ds_read_b128 v[80:83], v6 offset:27648\n"
"buffer_load_dwordx4 v6, s[16:19], s65 offen offset:1024 sc1 lds\n"
"v_exp_f32 v124, v124\n"
"v_mul_f32 v132, v36, v44\n"
"v_exp_f32 v125, v125\n"
"v_mfma_f32_32x32x16_bf16 v[64:79], v[92:95], a[96:99], v[64:79]\n"
"ds_read_b128 v[84:87], v6 offset:28672\n"
"v_mul_f32 v133, v37, v45\n"
"v_exp_f32 v126, v126\n"
"v_mul_f32 v134, v38, v46\n"
"v_exp_f32 v127, v127\n"
"s_waitcnt lgkmcnt(2)\n"
"v_mfma_f32_32x32x16_bf16 v[64:79], v[96:99], a[100:103], v[64:79]\n"
"ds_read_b128 v[88:91], v6 offset:29696\n"
"buffer_load_dwordx4 v6, s[16:19], s65 offen offset:2048 sc1 lds\n"
"v_mul_f32 v135, v39, v47\n"
"v_fma_f32 v124, v124, v13, v13\n"
"v_fma_f32 v125, v125, v13, v13\n"
"v_fma_f32 v126, v126, v13, v13\n"
"v_mfma_f32_32x32x16_bf16 v[64:79], v[100:103], a[104:107], v[64:79]\n"
"ds_read_b128 v[92:95], v6 offset:30720\n"
"v_fma_f32 v127, v127, v13, v13\n"
"v_rcp_f32 v124, v124\n"
"v_rcp_f32 v125, v125\n"
"s_waitcnt lgkmcnt(2)\n"
"v_mfma_f32_32x32x16_bf16 v[64:79], v[80:83], a[108:111], v[64:79]\n"
"ds_read_b128 v[96:99], v6 offset:31744\n"
"buffer_load_dwordx4 v6, s[16:19], s65 offen offset:3072 sc1 lds\n"
"v_rcp_f32 v126, v126\n"
"v_rcp_f32 v127, v127\n"
"v_mul_f32 v124, v124, v132\n"
"v_mfma_f32_32x32x16_bf16 v[64:79], v[84:87], a[112:115], v[64:79]\n"
"ds_read_b128 v[100:103], v14 offset:1536\n"
"v_mul_f32 v125, v125, v133\n"
"v_mul_f32 v126, v126, v134\n"
"v_mul_f32 v127, v127, v135\n"
"v_cvt_pk_bf16_f32 v142, v124, v125\n"
"v_cvt_pk_bf16_f32 v143, v126, v127\n"
"s_waitcnt lgkmcnt(2)\n"
"v_mfma_f32_32x32x16_bf16 v[64:79], v[88:91], a[116:119], v[64:79]\n"
"s_waitcnt vmcnt(8)\n"
"s_barrier\n"
"ds_read_b128 v[80:83], v7 offset:0\n"
"s_add_u32 m0, s75, 102400\n"
"s_add_u32 s65, s4, 163840\n"
"buffer_load_dwordx4 v6, s[16:19], s65 offen offset:0 sc1 lds\n"
"v_mfma_f32_32x32x16_bf16 v[64:79], v[92:95], a[120:123], v[64:79]\n"
"ds_read_b128 v[84:87], v7 offset:1024\n"
"buffer_store_dwordx4 v[136:139], v6, s[16:19], s74 offen sc1\n"
"s_add_u32 s74, s74, 4096\n"
"buffer_store_dwordx4 v[140:143], v6, s[16:19], s74 offen sc1\n"
"s_add_u32 s74, s74, 4096\n"
"s_waitcnt lgkmcnt(2)\n"
"v_mfma_f32_32x32x16_bf16 v[64:79], v[96:99], a[124:127], v[64:79]\n"
"ds_read_b128 v[88:91], v7 offset:2048\n"
"buffer_load_dwordx4 v6, s[16:19], s65 offen offset:1024 sc1 lds\n"
"v_mul_f32 v120, v12, v48\n"
"v_mul_f32 v121, v12, v49\n"
"v_mul_f32 v122, v12, v50\n"
"v_mul_f32 v123, v12, v51\n"
"v_exp_f32 v120, v120\n"
"v_mfma_f32_32x32x16_bf16 v[64:79], v[100:103], v[104:107], v[64:79]\n"
"ds_read_b128 v[92:95], v7 offset:3072\n"
"v_mul_f32 v128, v48, v56\n"
"v_exp_f32 v121, v121\n"
"v_mul_f32 v129, v49, v57\n"
"s_waitcnt lgkmcnt(2)\n"
"v_mfma_f32_32x32x16_bf16 v[16:31], v[80:83], a[0:3], 0\n"
"ds_read_b128 v[96:99], v7 offset:4096\n"
"buffer_load_dwordx4 v6, s[16:19], s65 offen offset:2048 sc1 lds\n"
"v_exp_f32 v122, v122\n"
"v_mul_f32 v130, v50, v58\n"
"v_exp_f32 v123, v123\n"
"v_mfma_f32_32x32x16_bf16 v[16:31], v[84:87], a[4:7], v[16:31]\n"
"ds_read_b128 v[100:103], v7 offset:5120\n"
"v_mul_f32 v131, v51, v59\n"
"v_fma_f32 v120, v120, v13, v13\n"
"v_fma_f32 v121, v121, v13, v13\n"
"v_fma_f32 v122, v122, v13, v13\n"
"v_fma_f32 v123, v123, v13, v13\n"
"s_waitcnt lgkmcnt(2)\n"
"v_mfma_f32_32x32x16_bf16 v[16:31], v[88:91], a[8:11], v[16:31]\n"
"ds_read_b128 v[80:83], v7 offset:6144\n"
"buffer_load_dwordx4 v6, s[16:19], s65 offen offset:3072 sc1 lds\n"
"v_rcp_f32 v120, v120\n"
"v_rcp_f32 v121, v121\n"
"v_mfma_f32_32x32x16_bf16 v[16:31], v[92:95], a[12:15], v[16:31]\n"
"ds_read_b128 v[84:87], v7 offset:7168\n"
"v_rcp_f32 v122, v122\n"
"v_rcp_f32 v123, v123\n"
"v_mul_f32 v120, v120, v128\n"
"s_waitcnt lgkmcnt(2)\n"
"v_mfma_f32_32x32x16_bf16 v[16:31], v[96:99], a[16:19], v[16:31]\n"
"ds_read_b128 v[88:91], v7 offset:8192\n"
"buffer_load_dwordx4 a[252:255], v3, s[16:19], s71 offen sc1\n"
"v_mul_f32 v121, v121, v129\n"
"v_mul_f32 v122, v122, v130\n"
"v_mul_f32 v123, v123, v131\n"
"v_cvt_pk_bf16_f32 v136, v120, v121\n"
"v_cvt_pk_bf16_f32 v137, v122, v123\n"
"v_mfma_f32_32x32x16_bf16 v[16:31], v[100:103], a[20:23], v[16:31]\n"
"ds_read_b128 v[92:95], v7 offset:9216\n"
"v_mul_f32 v124, v12, v52\n"
"v_mul_f32 v125, v12, v53\n"
"v_mul_f32 v126, v12, v54\n"
"v_mul_f32 v127, v12, v55\n"
"v_exp_f32 v124, v124\n"
"s_waitcnt lgkmcnt(2)\n"
"v_mfma_f32_32x32x16_bf16 v[16:31], v[80:83], a[24:27], v[16:31]\n"
"ds_read_b128 v[96:99], v7 offset:10240\n"
"s_mov_b32 s71, 32768\n"
"buffer_load_dwordx4 v[160:163], v2, s[16:19], s71 offen sc1\n"
"v_mul_f32 v132, v52, v60\n"
"v_exp_f32 v125, v125\n"
"v_mfma_f32_32x32x16_bf16 v[16:31], v[84:87], a[28:31], v[16:31]\n"
"ds_read_b128 v[100:103], v7 offset:11264\n"
"buffer_load_dwordx4 v[164:167], v3, s[16:19], s71 offen sc1\n"
"v_mul_f32 v133, v53, v61\n"
"v_exp_f32 v126, v126\n"
"v_mul_f32 v134, v54, v62\n"
"s_waitcnt lgkmcnt(2)\n"
"v_mfma_f32_32x32x16_bf16 v[16:31], v[88:91], a[32:35], v[16:31]\n"
"ds_read_b128 v[80:83], v7 offset:12288\n"
"v_exp_f32 v127, v127\n"
"v_mul_f32 v135, v55, v63\n"
"v_fma_f32 v124, v124, v13, v13\n"
"v_fma_f32 v125, v125, v13, v13\n"
"v_mfma_f32_32x32x16_bf16 v[16:31], v[92:95], a[36:39], v[16:31]\n"
"ds_read_b128 v[84:87], v7 offset:13312\n"
"s_mov_b32 s71, 34816\n"
"buffer_load_dwordx4 v[168:171], v2, s[16:19], s71 offen sc1\n"
"v_fma_f32 v126, v126, v13, v13\n"
"v_fma_f32 v127, v127, v13, v13\n"
"s_waitcnt lgkmcnt(2)\n"
"v_mfma_f32_32x32x16_bf16 v[16:31], v[96:99], a[40:43], v[16:31]\n"
"ds_read_b128 v[88:91], v7 offset:14336\n"
"v_rcp_f32 v124, v124\n"
"v_rcp_f32 v125, v125\n"
"v_rcp_f32 v126, v126\n"
"v_mfma_f32_32x32x16_bf16 v[16:31], v[100:103], a[44:47], v[16:31]\n"
"ds_read_b128 v[92:95], v7 offset:15360\n"
"buffer_load_dwordx4 v[172:175], v3, s[16:19], s71 offen sc1\n"
"v_rcp_f32 v127, v127\n"
"v_mul_f32 v124, v124, v132\n"
"v_mul_f32 v125, v125, v133\n"
"s_waitcnt lgkmcnt(2)\n"
"v_mfma_f32_32x32x16_bf16 v[16:31], v[80:83], a[48:51], v[16:31]\n"
"ds_read_b128 v[96:99], v7 offset:16384\n"
"v_mul_f32 v126, v126, v134\n"
"v_mul_f32 v127, v127, v135\n"
"v_cvt_pk_bf16_f32 v138, v124, v125\n"
"v_cvt_pk_bf16_f32 v139, v126, v127\n"
"v_mul_f32 v120, v12, v64\n"
"v_mul_f32 v121, v12, v65\n"
"v_mfma_f32_32x32x16_bf16 v[16:31], v[84:87], a[52:55], v[16:31]\n"
"ds_read_b128 v[100:103], v7 offset:17408\n"
"s_mov_b32 s71, 36864\n"
"buffer_load_dwordx4 v[176:179], v2, s[16:19], s71 offen sc1\n"
"v_mul_f32 v122, v12, v66\n"
"v_mul_f32 v123, v12, v67\n"
"v_exp_f32 v120, v120\n"
"s_waitcnt lgkmcnt(2)\n"
"v_mfma_f32_32x32x16_bf16 v[16:31], v[88:91], a[56:59], v[16:31]\n"
"ds_read_b128 v[80:83], v7 offset:18432\n"
"v_mul_f32 v128, v64, v72\n"
"v_exp_f32 v121, v121\n"
"v_mul_f32 v129, v65, v73\n"
"v_exp_f32 v122, v122\n"
"v_mfma_f32_32x32x16_bf16 v[16:31], v[92:95], a[60:63], v[16:31]\n"
"ds_read_b128 v[84:87], v7 offset:19456\n"
"buffer_load_dwordx4 v[180:183], v3, s[16:19], s71 offen sc1\n"
"v_mul_f32 v130, v66, v74\n"
"v_exp_f32 v123, v123\n"
"s_waitcnt lgkmcnt(2)\n"
"v_mfma_f32_32x32x16_bf16 v[16:31], v[96:99], a[64:67], v[16:31]\n"
"ds_read_b128 v[88:91], v7 offset:20480\n"
"v_mul_f32 v131, v67, v75\n"
"v_fma_f32 v120, v120, v13, v13\n"
"v_fma_f32 v121, v121, v13, v13\n"
"v_fma_f32 v122, v122, v13, v13\n"
"v_fma_f32 v123, v123, v13, v13\n"
"v_mfma_f32_32x32x16_bf16 v[16:31], v[100:103], a[68:71], v[16:31]\n"
"ds_read_b128 v[92:95], v7 offset:21504\n"
"s_mov_b32 s71, 38912\n"
"buffer_load_dwordx4 v[184:187], v2, s[16:19], s71 offen sc1\n"
"v_rcp_f32 v120, v120\n"
"v_rcp_f32 v121, v121\n"
"s_waitcnt lgkmcnt(2)\n"
"v_mfma_f32_32x32x16_bf16 v[16:31], v[80:83], a[72:75], v[16:31]\n"
"ds_read_b128 v[96:99], v7 offset:22528\n"
"v_rcp_f32 v122, v122\n"
"v_rcp_f32 v123, v123\n"
"v_mul_f32 v120, v120, v128\n"
"v_mfma_f32_32x32x16_bf16 v[16:31], v[84:87], a[76:79], v[16:31]\n"
"ds_read_b128 v[100:103], v7 offset:23552\n"
"s_add_u32 m0, s75, 131072\n"
"s_add_u32 s65, s63, 196608\n"
"buffer_load_dwordx4 v6, s[16:19], s65 offen offset:0 sc1 lds\n"
"v_mul_f32 v121, v121, v129\n"
"v_mul_f32 v122, v122, v130\n"
"s_waitcnt lgkmcnt(2)\n"
"v_mfma_f32_32x32x16_bf16 v[16:31], v[88:91], a[80:83], v[16:31]\n"
"ds_read_b128 v[80:83], v7 offset:24576\n"
"v_mul_f32 v123, v123, v131\n"
"v_cvt_pk_bf16_f32 v140, v120, v121\n"
"v_cvt_pk_bf16_f32 v141, v122, v123\n"
"v_mul_f32 v124, v12, v68\n"
"v_mul_f32 v125, v12, v69\n"
"v_mfma_f32_32x32x16_bf16 v[16:31], v[92:95], a[84:87], v[16:31]\n"
"ds_read_b128 v[84:87], v7 offset:25600\n"
"buffer_load_dwordx4 v6, s[16:19], s65 offen offset:1024 sc1 lds\n"
"v_mul_f32 v126, v12, v70\n"
"v_mul_f32 v127, v12, v71\n"
"v_exp_f32 v124, v124\n"
"s_waitcnt lgkmcnt(2)\n"
"v_mfma_f32_32x32x16_bf16 v[16:31], v[96:99], a[88:91], v[16:31]\n"
"ds_read_b128 v[88:91], v7 offset:26624\n"
"v_mul_f32 v132, v68, v76\n"
"v_exp_f32 v125, v125\n"
"v_mul_f32 v133, v69, v77\n"
"v_exp_f32 v126, v126\n"
"v_mfma_f32_32x32x16_bf16 v[16:31], v[100:103], a[92:95], v[16:31]\n"
"ds_read_b128 v[92:95], v7 offset:27648\n"
"buffer_load_dwordx4 v6, s[16:19], s65 offen offset:2048 sc1 lds\n"
"v_mul_f32 v134, v70, v78\n"
"v_exp_f32 v127, v127\n"
"v_mul_f32 v135, v71, v79\n"
"s_waitcnt lgkmcnt(2)\n"
"v_mfma_f32_32x32x16_bf16 v[16:31], v[80:83], a[96:99], v[16:31]\n"
"ds_read_b128 v[96:99], v7 offset:28672\n"
"v_fma_f32 v124, v124, v13, v13\n"
"v_fma_f32 v125, v125, v13, v13\n"
"v_fma_f32 v126, v126, v13, v13\n"
"v_fma_f32 v127, v127, v13, v13\n"
"v_rcp_f32 v124, v124\n"
"v_mfma_f32_32x32x16_bf16 v[16:31], v[84:87], a[100:103], v[16:31]\n"
"ds_read_b128 v[100:103], v7 offset:29696\n"
"buffer_load_dwordx4 v6, s[16:19], s65 offen offset:3072 sc1 lds\n"
"v_rcp_f32 v125, v125\n"
"v_rcp_f32 v126, v126\n"
"s_waitcnt lgkmcnt(2)\n"
"v_mfma_f32_32x32x16_bf16 v[16:31], v[88:91], a[104:107], v[16:31]\n"
"ds_read_b128 v[80:83], v7 offset:30720\n"
"s_add_u32 m0, s75, 135168\n"
"s_add_u32 s65, s4, 196608\n"
"buffer_load_dwordx4 v6, s[16:19], s65 offen offset:0 sc1 lds\n"
"v_rcp_f32 v127, v127\n"
"v_mfma_f32_32x32x16_bf16 v[16:31], v[92:95], a[108:111], v[16:31]\n"
"ds_read_b128 v[84:87], v7 offset:31744\n"
"v_mul_f32 v124, v124, v132\n"
"v_mul_f32 v125, v125, v133\n"
"v_mul_f32 v126, v126, v134\n"
"v_mul_f32 v127, v127, v135\n"
"v_cvt_pk_bf16_f32 v142, v124, v125\n"
"s_waitcnt lgkmcnt(2)\n"
"v_mfma_f32_32x32x16_bf16 v[16:31], v[96:99], a[112:115], v[16:31]\n"
"ds_read_b128 v[88:91], v14 offset:2048\n"
"buffer_load_dwordx4 v6, s[16:19], s65 offen offset:1024 sc1 lds\n"
"v_cvt_pk_bf16_f32 v143, v126, v127\n"
"s_waitcnt vmcnt(17)\n"
"s_mov_b64 exec, 1\n"
"buffer_store_dword v9, off, s[16:19], s6 offset:0 sc1\n"
"s_mov_b64 exec, -1\n"
"v_mfma_f32_32x32x16_bf16 v[16:31], v[100:103], a[116:119], v[16:31]\n"
"s_waitcnt vmcnt(15)\n"
"s_barrier\n"
"ds_read_b128 v[92:95], v7 offset:32768\n"
"buffer_store_dwordx4 v[136:139], v6, s[16:19], s74 offen sc1\n"
"s_add_u32 s74, s74, 4096\n"
"s_waitcnt lgkmcnt(2)\n"
"v_mfma_f32_32x32x16_bf16 v[16:31], v[80:83], a[120:123], v[16:31]\n"
"ds_read_b128 v[96:99], v7 offset:33792\n"
"buffer_load_dwordx4 v6, s[16:19], s65 offen offset:2048 sc1 lds\n"
"buffer_store_dwordx4 v[140:143], v6, s[16:19], s74 offen sc1\n"
"s_add_u32 s74, s74, 4096\n"
"v_mfma_f32_32x32x16_bf16 v[16:31], v[84:87], a[124:127], v[16:31]\n"
"ds_read_b128 v[100:103], v7 offset:34816\n"
"s_waitcnt lgkmcnt(2)\n"
"v_mfma_f32_32x32x16_bf16 v[16:31], v[88:91], v[104:107], v[16:31]\n"
"ds_read_b128 v[80:83], v7 offset:35840\n"
"buffer_load_dwordx4 v6, s[16:19], s65 offen offset:3072 sc1 lds\n"
"v_mfma_f32_32x32x16_bf16 v[32:47], v[92:95], a[0:3], 0\n"
"ds_read_b128 v[84:87], v7 offset:36864\n"
"s_waitcnt lgkmcnt(2)\n"
"v_mfma_f32_32x32x16_bf16 v[32:47], v[96:99], a[4:7], v[32:47]\n"
"ds_read_b128 v[88:91], v7 offset:37888\n"
"buffer_load_dwordx4 v[188:191], v3, s[16:19], s71 offen sc1\n"
"s_nop 3\n"
"v_mul_f32 v120, v12, v16\n"
"v_mul_f32 v121, v12, v17\n"
"v_mul_f32 v122, v12, v18\n"
"v_mul_f32 v123, v12, v19\n"
"v_exp_f32 v120, v120\n"
"v_mfma_f32_32x32x16_bf16 v[32:47], v[100:103], a[8:11], v[32:47]\n"
"ds_read_b128 v[92:95], v7 offset:38912\n"
"v_mul_f32 v128, v16, v24\n"
"v_exp_f32 v121, v121\n"
"v_mul_f32 v129, v17, v25\n"
"s_waitcnt lgkmcnt(2)\n"
"v_mfma_f32_32x32x16_bf16 v[32:47], v[80:83], a[12:15], v[32:47]\n"
"ds_read_b128 v[96:99], v7 offset:39936\n"
"s_mov_b32 s71, 40960\n"
"buffer_load_dwordx4 v[192:195], v2, s[16:19], s71 offen sc1\n"
"v_exp_f32 v122, v122\n"
"v_mul_f32 v130, v18, v26\n"
"v_exp_f32 v123, v123\n"
"v_mfma_f32_32x32x16_bf16 v[32:47], v[84:87], a[16:19], v[32:47]\n"
"ds_read_b128 v[100:103], v7 offset:40960\n"
"v_mul_f32 v131, v19, v27\n"
"v_fma_f32 v120, v120, v13, v13\n"
"v_fma_f32 v121, v121, v13, v13\n"
"v_fma_f32 v122, v122, v13, v13\n"
"s_waitcnt lgkmcnt(2)\n"
"v_mfma_f32_32x32x16_bf16 v[32:47], v[88:91], a[20:23], v[32:47]\n"
"ds_read_b128 v[80:83], v7 offset:41984\n"
"buffer_load_dwordx4 v[196:199], v3, s[16:19], s71 offen sc1\n"
"v_fma_f32 v123, v123, v13, v13\n"
"v_rcp_f32 v120, v120\n"
"v_rcp_f32 v121, v121\n"
"v_mfma_f32_32x32x16_bf16 v[32:47], v[92:95], a[24:27], v[32:47]\n"
"ds_read_b128 v[84:87], v7 offset:43008\n"
"v_rcp_f32 v122, v122\n"
"v_rcp_f32 v123, v123\n"
"s_waitcnt lgkmcnt(2)\n"
"v_mfma_f32_32x32x16_bf16 v[32:47], v[96:99], a[28:31], v[32:47]\n"
"ds_read_b128 v[88:91], v7 offset:44032\n"
"s_mov_b32 s71, 43008\n"
"buffer_load_dwordx4 v[200:203], v2, s[16:19], s71 offen sc1\n"
"v_mul_f32 v120, v120, v128\n"
"v_mul_f32 v121, v121, v129\n"
"v_mul_f32 v122, v122, v130\n"
"v_mul_f32 v123, v123, v131\n"
"v_mfma_f32_32x32x16_bf16 v[32:47], v[100:103], a[32:35], v[32:47]\n"
"ds_read_b128 v[92:95], v7 offset:45056\n"
"v_cvt_pk_bf16_f32 v136, v120, v121\n"
"v_cvt_pk_bf16_f32 v137, v122, v123\n"
"v_mul_f32 v124, v12, v20\n"
"v_mul_f32 v125, v12, v21\n"
"v_mul_f32 v126, v12, v22\n"
"s_waitcnt lgkmcnt(2)\n"
"v_mfma_f32_32x32x16_bf16 v[32:47], v[80:83], a[36:39], v[32:47]\n"
"ds_read_b128 v[96:99], v7 offset:46080\n"
"buffer_load_dwordx4 v[204:207], v3, s[16:19], s71 offen sc1\n"
"v_mul_f32 v127, v12, v23\n"
"v_exp_f32 v124, v124\n"
"v_mul_f32 v132, v20, v28\n"
"v_exp_f32 v125, v125\n"
"v_mfma_f32_32x32x16_bf16 v[32:47], v[84:87], a[40:43], v[32:47]\n"
"ds_read_b128 v[100:103], v7 offset:47104\n"
"v_mul_f32 v133, v21, v29\n"
"v_exp_f32 v126, v126\n"
"v_mul_f32 v134, v22, v30\n"
"s_waitcnt lgkmcnt(2)\n"
"v_mfma_f32_32x32x16_bf16 v[32:47], v[88:91], a[44:47], v[32:47]\n"
"ds_read_b128 v[80:83], v7 offset:48128\n"
"s_mov_b32 s71, 45056\n"
"buffer_load_dwordx4 v[208:211], v2, s[16:19], s71 offen sc1\n"
"v_exp_f32 v127, v127\n"
"v_mul_f32 v135, v23, v31\n"
"v_fma_f32 v124, v124, v13, v13\n"
"v_mfma_f32_32x32x16_bf16 v[32:47], v[92:95], a[48:51], v[32:47]\n"
"ds_read_b128 v[84:87], v7 offset:49152\n"
"buffer_load_dwordx4 v[212:215], v3, s[16:19], s71 offen sc1\n"
"v_fma_f32 v125, v125, v13, v13\n"
"v_fma_f32 v126, v126, v13, v13\n"
"v_fma_f32 v127, v127, v13, v13\n"
"s_waitcnt lgkmcnt(2)\n"
"v_mfma_f32_32x32x16_bf16 v[32:47], v[96:99], a[52:55], v[32:47]\n"
"ds_read_b128 v[88:91], v7 offset:50176\n"
"v_rcp_f32 v124, v124\n"
"v_rcp_f32 v125, v125\n"
"v_rcp_f32 v126, v126\n"
"v_mfma_f32_32x32x16_bf16 v[32:47], v[100:103], a[56:59], v[32:47]\n"
"ds_read_b128 v[92:95], v7 offset:51200\n"
"s_mov_b32 s71, 47104\n"
"buffer_load_dwordx4 v[216:219], v2, s[16:19], s71 offen sc1\n"
"v_rcp_f32 v127, v127\n"
"v_mul_f32 v124, v124, v132\n"
"s_waitcnt lgkmcnt(2)\n"
"v_mfma_f32_32x32x16_bf16 v[32:47], v[80:83], a[60:63], v[32:47]\n"
"ds_read_b128 v[96:99], v7 offset:52224\n"
"v_mul_f32 v125, v125, v133\n"
"v_mul_f32 v126, v126, v134\n"
"v_mul_f32 v127, v127, v135\n"
"v_cvt_pk_bf16_f32 v138, v124, v125\n"
"v_cvt_pk_bf16_f32 v139, v126, v127\n"
"v_mfma_f32_32x32x16_bf16 v[32:47], v[84:87], a[64:67], v[32:47]\n"
"ds_read_b128 v[100:103], v7 offset:53248\n"
"buffer_load_dwordx4 v[220:223], v3, s[16:19], s71 offen sc1\n"
"buffer_store_dwordx4 v[136:139], v6, s[16:19], s74 offen sc1\n"
"s_add_u32 s74, s74, 4096\n"
"s_waitcnt lgkmcnt(2)\n"
"v_mfma_f32_32x32x16_bf16 v[32:47], v[88:91], a[68:71], v[32:47]\n"
"ds_read_b128 v[80:83], v7 offset:54272\n"
"v_mfma_f32_32x32x16_bf16 v[32:47], v[92:95], a[72:75], v[32:47]\n"
"ds_read_b128 v[84:87], v7 offset:55296\n"
"s_mov_b32 s71, 49152\n"
"buffer_load_dwordx4 v[224:227], v2, s[16:19], s71 offen sc1\n"
"s_waitcnt lgkmcnt(2)\n"
"v_mfma_f32_32x32x16_bf16 v[32:47], v[96:99], a[76:79], v[32:47]\n"
"ds_read_b128 v[88:91], v7 offset:56320\n"
"buffer_load_dwordx4 v[112:115], v110, s[16:19], s7 offen sc1\n"
"v_mfma_f32_32x32x16_bf16 v[32:47], v[100:103], a[80:83], v[32:47]\n"
"ds_read_b128 v[92:95], v7 offset:57344\n"
"buffer_load_dwordx4 v[228:231], v3, s[16:19], s71 offen sc1\n"
"s_waitcnt lgkmcnt(2)\n"
"v_mfma_f32_32x32x16_bf16 v[32:47], v[80:83], a[84:87], v[32:47]\n"
"ds_read_b128 v[96:99], v7 offset:58368\n"
"v_mfma_f32_32x32x16_bf16 v[32:47], v[84:87], a[88:91], v[32:47]\n"
"ds_read_b128 v[100:103], v7 offset:59392\n"
"s_mov_b32 s71, 51200\n"
"buffer_load_dwordx4 v[232:235], v2, s[16:19], s71 offen sc1\n"
"s_waitcnt lgkmcnt(2)\n"
"v_mfma_f32_32x32x16_bf16 v[32:47], v[88:91], a[92:95], v[32:47]\n"
"ds_read_b128 v[80:83], v7 offset:60416\n"
"v_mfma_f32_32x32x16_bf16 v[32:47], v[92:95], a[96:99], v[32:47]\n"
"ds_read_b128 v[84:87], v7 offset:61440\n"
"buffer_load_dwordx4 v[236:239], v3, s[16:19], s71 offen sc1\n"
"s_waitcnt lgkmcnt(2)\n"
"v_mfma_f32_32x32x16_bf16 v[32:47], v[96:99], a[100:103], v[32:47]\n"
"ds_read_b128 v[88:91], v7 offset:62464\n"
"v_mfma_f32_32x32x16_bf16 v[32:47], v[100:103], a[104:107], v[32:47]\n"
"ds_read_b128 v[92:95], v7 offset:63488\n"
"s_mov_b32 s71, 53248\n"
"buffer_load_dwordx4 v[240:243], v2, s[16:19], s71 offen sc1\n"
"s_waitcnt lgkmcnt(2)\n"
"v_mfma_f32_32x32x16_bf16 v[32:47], v[80:83], a[108:111], v[32:47]\n"
"ds_read_b128 v[96:99], v7 offset:64512\n"
"v_mfma_f32_32x32x16_bf16 v[32:47], v[84:87], a[112:115], v[32:47]\n"
"ds_read_b128 v[100:103], v14 offset:2560\n"
"buffer_load_dwordx4 v[244:247], v3, s[16:19], s71 offen sc1\n"
"s_waitcnt lgkmcnt(2)\n"
"v_mfma_f32_32x32x16_bf16 v[32:47], v[88:91], a[116:119], v[32:47]\n"
"s_waitcnt vmcnt(17)\n"
"s_barrier\n"
"ds_read_b128 v[80:83], v8 offset:0\n"
"v_mfma_f32_32x32x16_bf16 v[32:47], v[92:95], a[120:123], v[32:47]\n"
"ds_read_b128 v[84:87], v8 offset:1024\n"
"s_mov_b32 s71, 55296\n"
"buffer_load_dwordx4 v[248:251], v2, s[16:19], s71 offen sc1\n"
"s_waitcnt lgkmcnt(2)\n"
"v_mfma_f32_32x32x16_bf16 v[32:47], v[96:99], a[124:127], v[32:47]\n"
"ds_read_b128 v[88:91], v8 offset:2048\n"
"buffer_load_dwordx4 v[252:255], v3, s[16:19], s71 offen sc1\n"
"v_mfma_f32_32x32x16_bf16 v[32:47], v[100:103], v[104:107], v[32:47]\n"
"ds_read_b128 v[92:95], v8 offset:3072\n"
"s_waitcnt lgkmcnt(2)\n"
"v_mfma_f32_32x32x16_bf16 v[48:63], v[80:83], a[0:3], 0\n"
"ds_read_b128 v[96:99], v8 offset:4096\n"
"v_mfma_f32_32x32x16_bf16 v[48:63], v[84:87], a[4:7], v[48:63]\n"
"ds_read_b128 v[100:103], v8 offset:5120\n"
"s_nop 5\n"
"v_mul_f32 v120, v12, v32\n"
"v_mul_f32 v121, v12, v33\n"
"v_mul_f32 v122, v12, v34\n"
"v_mul_f32 v123, v12, v35\n"
"v_exp_f32 v120, v120\n"
"s_waitcnt lgkmcnt(2)\n"
"v_mfma_f32_32x32x16_bf16 v[48:63], v[88:91], a[8:11], v[48:63]\n"
"ds_read_b128 v[80:83], v8 offset:6144\n"
"v_mul_f32 v128, v32, v40\n"
"v_exp_f32 v121, v121\n"
"v_mul_f32 v129, v33, v41\n"
"v_exp_f32 v122, v122\n"
"v_mfma_f32_32x32x16_bf16 v[48:63], v[92:95], a[12:15], v[48:63]\n"
"ds_read_b128 v[84:87], v8 offset:7168\n"
"v_mul_f32 v130, v34, v42\n"
"v_exp_f32 v123, v123\n"
"v_mul_f32 v131, v35, v43\n"
"v_fma_f32 v120, v120, v13, v13\n"
"s_waitcnt lgkmcnt(2)\n"
"v_mfma_f32_32x32x16_bf16 v[48:63], v[96:99], a[16:19], v[48:63]\n"
"ds_read_b128 v[88:91], v8 offset:8192\n"
"v_fma_f32 v121, v121, v13, v13\n"
"v_fma_f32 v122, v122, v13, v13\n"
"v_fma_f32 v123, v123, v13, v13\n"
"v_rcp_f32 v120, v120\n"
"v_mfma_f32_32x32x16_bf16 v[48:63], v[100:103], a[20:23], v[48:63]\n"
"ds_read_b128 v[92:95], v8 offset:9216\n"
"v_rcp_f32 v121, v121\n"
"v_rcp_f32 v122, v122\n"
"v_rcp_f32 v123, v123\n"
"s_waitcnt lgkmcnt(2)\n"
"v_mfma_f32_32x32x16_bf16 v[48:63], v[80:83], a[24:27], v[48:63]\n"
"ds_read_b128 v[96:99], v8 offset:10240\n"
"s_waitcnt vmcnt(7)\n"
"v_cmp_ne_u32 vcc, s44, v112\n"
"s_cbranch_vccz .LK_lv0ok_10\n"
"s_mov_b32 s53, 32768\n"
".LK_lv0sp_11:\n"
"buffer_load_dwordx4 v[112:115], v110, s[16:19], s7 offen sc1\n"
"s_waitcnt vmcnt(0)\n"
"v_cmp_ne_u32 vcc, s44, v112\n"
"s_cbranch_vccz .LK_lv0ok_10\n"
"s_sub_u32 s53, s53, 1\n"
"s_cbranch_scc0 .LK_lv0sp_11\n"
".LK_lv0ok_10:\n"
"s_lshl_b32 s72, s70, 11\n"
"buffer_load_dwordx4 a[0:3], v108, s[16:19], s72 offen sc1\n"
"v_mfma_f32_32x32x16_bf16 v[48:63], v[84:87], a[28:31], v[48:63]\n"
"s_mov_b64 exec, 1\n"
"buffer_store_dword v9, off, s[16:19], s6 offset:4 sc1\n"
"s_mov_b64 exec, -1\n"
"ds_read_b128 v[100:103], v8 offset:11264\n"
"v_mul_f32 v120, v120, v128\n"
"v_mul_f32 v121, v121, v129\n"
"v_mul_f32 v122, v122, v130\n"
"s_waitcnt lgkmcnt(2)\n"
"v_mfma_f32_32x32x16_bf16 v[48:63], v[88:91], a[32:35], v[48:63]\n"
"ds_read_b128 v[80:83], v8 offset:12288\n"
"buffer_load_dwordx4 a[4:7], v109, s[16:19], s72 offen sc1\n"
"v_mul_f32 v123, v123, v131\n"
"v_cvt_pk_bf16_f32 v140, v120, v121\n"
"v_cvt_pk_bf16_f32 v141, v122, v123\n"
"v_mul_f32 v124, v12, v36\n"
"v_mul_f32 v125, v12, v37\n"
"v_mfma_f32_32x32x16_bf16 v[48:63], v[92:95], a[36:39], v[48:63]\n"
"ds_read_b128 v[84:87], v8 offset:13312\n"
"v_mul_f32 v126, v12, v38\n"
"v_mul_f32 v127, v12, v39\n"
"v_exp_f32 v124, v124\n"
"v_mul_f32 v132, v36, v44\n"
"s_waitcnt lgkmcnt(2)\n"
"v_mfma_f32_32x32x16_bf16 v[48:63], v[96:99], a[40:43], v[48:63]\n"
"ds_read_b128 v[88:91], v8 offset:14336\n"
"s_add_u32 s72, s72, 1024\n"
"s_and_b32 s72, s72, 16383\n"
"buffer_load_dwordx4 a[8:11], v108, s[16:19], s72 offen sc1\n"
"v_exp_f32 v125, v125\n"
"v_mfma_f32_32x32x16_bf16 v[48:63], v[100:103], a[44:47], v[48:63]\n"
"ds_read_b128 v[92:95], v8 offset:15360\n"
"v_mul_f32 v133, v37, v45\n"
"v_exp_f32 v126, v126\n"
"v_mul_f32 v134, v38, v46\n"
"v_exp_f32 v127, v127\n"
"s_waitcnt lgkmcnt(2)\n"
"v_mfma_f32_32x32x16_bf16 v[48:63], v[80:83], a[48:51], v[48:63]\n"
"ds_read_b128 v[96:99], v8 offset:16384\n"
"buffer_load_dwordx4 a[12:15], v109, s[16:19], s72 offen sc1\n"
"v_mul_f32 v135, v39, v47\n"
"v_fma_f32 v124, v124, v13, v13\n"
"v_fma_f32 v125, v125, v13, v13\n"
"v_fma_f32 v126, v126, v13, v13\n"
"v_mfma_f32_32x32x16_bf16 v[48:63], v[84:87], a[52:55], v[48:63]\n"
"ds_read_b128 v[100:103], v8 offset:17408\n"
"v_fma_f32 v127, v127, v13, v13\n"
"v_rcp_f32 v124, v124\n"
"v_rcp_f32 v125, v125\n"
"s_waitcnt lgkmcnt(2)\n"
"v_mfma_f32_32x32x16_bf16 v[48:63], v[88:91], a[56:59], v[48:63]\n"
"ds_read_b128 v[80:83], v8 offset:18432\n"
"s_add_u32 s72, s72, 1024\n"
"s_and_b32 s72, s72, 16383\n"
"buffer_load_dwordx4 a[16:19], v108, s[16:19], s72 offen sc1\n"
"v_rcp_f32 v126, v126\n"
"v_mfma_f32_32x32x16_bf16 v[48:63], v[92:95], a[60:63], v[48:63]\n"
"ds_read_b128 v[84:87], v8 offset:19456\n"
"v_rcp_f32 v127, v127\n"
"v_mul_f32 v124, v124, v132\n"
"v_mul_f32 v125, v125, v133\n"
"v_mul_f32 v126, v126, v134\n"
"s_waitcnt lgkmcnt(2)\n"
"v_mfma_f32_32x32x16_bf16 v[48:63], v[96:99], a[64:67], v[48:63]\n"
"ds_read_b128 v[88:91], v8 offset:20480\n"
"buffer_load_dwordx4 a[20:23], v109, s[16:19], s72 offen sc1\n"
"v_mul_f32 v127, v127, v135\n"
"v_cvt_pk_bf16_f32 v142, v124, v125\n"
"v_cvt_pk_bf16_f32 v143, v126, v127\n"
"buffer_store_dwordx4 v[140:143], v6, s[16:19], s74 offen sc1\n"
"v_mfma_f32_32x32x16_bf16 v[48:63], v[100:103], a[68:71], v[48:63]\n"
"ds_read_b128 v[92:95], v8 offset:21504\n"
"s_add_u32 s72, s72, 1024\n"
"s_and_b32 s72, s72, 16383\n"
"buffer_load_dwordx4 a[24:27], v108, s[16:19], s72 offen sc1\n"
"s_add_u32 s74, s74, 4096\n"
"s_waitcnt lgkmcnt(2)\n"
"v_mfma_f32_32x32x16_bf16 v[48:63], v[80:83], a[72:75], v[48:63]\n"
"ds_read_b128 v[96:99], v8 offset:22528\n"
"v_mfma_f32_32x32x16_bf16 v[48:63], v[84:87], a[76:79], v[48:63]\n"
"ds_read_b128 v[100:103], v8 offset:23552\n"
"buffer_load_dwordx4 a[28:31], v109, s[16:19], s72 offen sc1\n"
"s_waitcnt lgkmcnt(2)\n"
"v_mfma_f32_32x32x16_bf16 v[48:63], v[88:91], a[80:83], v[48:63]\n"
"ds_read_b128 v[80:83], v8 offset:24576\n"
"v_mfma_f32_32x32x16_bf16 v[48:63], v[92:95], a[84:87], v[48:63]\n"
"ds_read_b128 v[84:87], v8 offset:25600\n"
"s_add_u32 s72, s72, 1024\n"
"s_and_b32 s72, s72, 16383\n"
"buffer_load_dwordx4 a[32:35], v108, s[16:19], s72 offen sc1\n"
"s_waitcnt lgkmcnt(2)\n"
"v_mfma_f32_32x32x16_bf16 v[48:63], v[96:99], a[88:91], v[48:63]\n"
"ds_read_b128 v[88:91], v8 offset:26624\n"
"v_mfma_f32_32x32x16_bf16 v[48:63], v[100:103], a[92:95], v[48:63]\n"
"ds_read_b128 v[92:95], v8 offset:27648\n"
"buffer_load_dwordx4 a[36:39], v109, s[16:19], s72 offen sc1\n"
"s_waitcnt lgkmcnt(2)\n"
"v_mfma_f32_32x32x16_bf16 v[48:63], v[80:83], a[96:99], v[48:63]\n"
"ds_read_b128 v[96:99], v8 offset:28672\n"
"v_mfma_f32_32x32x16_bf16 v[48:63], v[84:87], a[100:103], v[48:63]\n"
"ds_read_b128 v[100:103], v8 offset:29696\n"
"s_add_u32 s72, s72, 1024\n"
"s_and_b32 s72, s72, 16383\n"
"buffer_load_dwordx4 a[40:43], v108, s[16:19], s72 offen sc1\n"
"s_waitcnt lgkmcnt(2)\n"
"v_mfma_f32_32x32x16_bf16 v[48:63], v[88:91], a[104:107], v[48:63]\n"
"ds_read_b128 v[80:83], v8 offset:30720\n"
"v_mfma_f32_32x32x16_bf16 v[48:63], v[92:95], a[108:111], v[48:63]\n"
"ds_read_b128 v[84:87], v8 offset:31744\n"
"buffer_load_dwordx4 a[44:47], v109, s[16:19], s72 offen sc1\n"
"s_waitcnt lgkmcnt(2)\n"
"v_mfma_f32_32x32x16_bf16 v[48:63], v[96:99], a[112:115], v[48:63]\n"
"ds_read_b128 v[88:91], v14 offset:3072\n"
"v_mfma_f32_32x32x16_bf16 v[48:63], v[100:103], a[116:119], v[48:63]\n"
"s_add_u32 s72, s72, 1024\n"
"s_and_b32 s72, s72, 16383\n"
"buffer_load_dwordx4 a[48:51], v108, s[16:19], s72 offen sc1\n"
"s_waitcnt lgkmcnt(1)\n"
"v_mfma_f32_32x32x16_bf16 v[48:63], v[80:83], a[120:123], v[48:63]\n"
"v_mfma_f32_32x32x16_bf16 v[48:63], v[84:87], a[124:127], v[48:63]\n"
"buffer_load_dwordx4 a[52:55], v109, s[16:19], s72 offen sc1\n"
"s_waitcnt lgkmcnt(0)\n"
"v_mfma_f32_32x32x16_bf16 v[48:63], v[88:91], v[104:107], v[48:63]\n"
"s_nop 11\n"
"v_mul_f32 v120, v12, v48\n"
"v_mul_f32 v121, v12, v49\n"
"v_mul_f32 v122, v12, v50\n"
"v_mul_f32 v123, v12, v51\n"
"s_add_u32 s72, s72, 1024\n"
"s_and_b32 s72, s72, 16383\n"
"buffer_load_dwordx4 a[56:59], v108, s[16:19], s72 offen sc1\n"
"v_exp_f32 v120, v120\n"
"v_mul_f32 v128, v48, v56\n"
"v_exp_f32 v121, v121\n"
"v_mul_f32 v129, v49, v57\n"
"buffer_load_dwordx4 a[60:63], v109, s[16:19], s72 offen sc1\n"
"v_exp_f32 v122, v122\n"
"v_mul_f32 v130, v50, v58\n"
"v_exp_f32 v123, v123\n"
"v_mul_f32 v131, v51, v59\n"
"v_fma_f32 v120, v120, v13, v13\n"
"v_fma_f32 v121, v121, v13, v13\n"
"v_fma_f32 v122, v122, v13, v13\n"
"v_fma_f32 v123, v123, v13, v13\n"
"v_rcp_f32 v120, v120\n"
"v_rcp_f32 v121, v121\n"
"v_rcp_f32 v122, v122\n"
"v_rcp_f32 v123, v123\n"
"v_mul_f32 v120, v120, v128\n"
"v_mul_f32 v121, v121, v129\n"
"v_mul_f32 v122, v122, v130\n"
"v_mul_f32 v123, v123, v131\n"
"v_cvt_pk_bf16_f32 v136, v120, v121\n"
"v_cvt_pk_bf16_f32 v137, v122, v123\n"
"v_mul_f32 v124, v12, v52\n"
"v_mul_f32 v125, v12, v53\n"
"v_mul_f32 v126, v12, v54\n"
"v_mul_f32 v127, v12, v55\n"
"v_exp_f32 v124, v124\n"
"v_mul_f32 v132, v52, v60\n"
"v_exp_f32 v125, v125\n"
"v_mul_f32 v133, v53, v61\n"
"v_exp_f32 v126, v126\n"
"v_mul_f32 v134, v54, v62\n"
"v_exp_f32 v127, v127\n"
"v_mul_f32 v135, v55, v63\n"
"v_fma_f32 v124, v124, v13, v13\n"
"v_fma_f32 v125, v125, v13, v13\n"
"v_fma_f32 v126, v126, v13, v13\n"
"v_fma_f32 v127, v127, v13, v13\n"
"v_rcp_f32 v124, v124\n"
"v_rcp_f32 v125, v125\n"
"v_rcp_f32 v126, v126\n"
"v_rcp_f32 v127, v127\n"
"v_mul_f32 v124, v124, v132\n"
"v_mul_f32 v125, v125, v133\n"
"v_mul_f32 v126, v126, v134\n"
"v_mul_f32 v127, v127, v135\n"
"v_cvt_pk_bf16_f32 v138, v124, v125\n"
"v_cvt_pk_bf16_f32 v139, v126, v127\n"
"buffer_load_dwordx4 v[112:115], v110, s[16:19], s7 offen sc1\n"
"buffer_store_dwordx4 v[136:139], v6, s[16:19], s74 offen sc1\n"
"s_waitcnt vmcnt(19)\n"
"v_mfma_f32_32x32x16_bf16 v[16:31], a[128:131], a[0:3], 0\n"
"v_mfma_f32_32x32x16_bf16 v[32:47], a[132:135], a[0:3], 0\n"
"s_waitcnt vmcnt(17)\n"
"v_mfma_f32_32x32x16_bf16 v[48:63], a[128:131], a[4:7], 0\n"
"v_mfma_f32_32x32x16_bf16 v[64:79], a[132:135], a[4:7], 0\n"
"s_waitcnt vmcnt(16)\n"
"v_mfma_f32_32x32x16_bf16 v[16:31], a[136:139], a[8:11], v[16:31]\n"
"v_mfma_f32_32x32x16_bf16 v[32:47], a[140:143], a[8:11], v[32:47]\n"
"s_waitcnt vmcnt(15)\n"
"v_mfma_f32_32x32x16_bf16 v[48:63], a[136:139], a[12:15], v[48:63]\n"
"v_mfma_f32_32x32x16_bf16 v[64:79], a[140:143], a[12:15], v[64:79]\n"
"s_waitcnt vmcnt(14)\n"
"v_mfma_f32_32x32x16_bf16 v[16:31], a[144:147], a[16:19], v[16:31]\n"
"v_mfma_f32_32x32x16_bf16 v[32:47], a[148:151], a[16:19], v[32:47]\n"
"s_waitcnt vmcnt(13)\n"
"v_mfma_f32_32x32x16_bf16 v[48:63], a[144:147], a[20:23], v[48:63]\n"
"s_waitcnt vmcnt(1)\n"
"v_cmp_ne_u32 vcc, s44, v113\n"
"s_cbranch_vccz .LK_lv1ok_12\n"
"s_mov_b32 s53, 32768\n"
".LK_lv1sp_13:\n"
"buffer_load_dwordx4 v[112:115], v110, s[16:19], s7 offen sc1\n"
"s_waitcnt vmcnt(0)\n"
"v_cmp_ne_u32 vcc, s44, v113\n"
"s_cbranch_vccz .LK_lv1ok_12\n"
"s_sub_u32 s53, s53, 1\n"
"s_cbranch_scc0 .LK_lv1sp_13\n"
".LK_lv1ok_12:\n"
"s_add_u32 s72, s72, 1024\n"
"s_and_b32 s72, s72, 16383\n"
"buffer_load_dwordx4 a[64:67], v108, s[16:19], s72 offen sc1\n"
"v_mfma_f32_32x32x16_bf16 v[64:79], a[148:151], a[20:23], v[64:79]\n"
"buffer_load_dwordx4 a[68:71], v109, s[16:19], s72 offen sc1\n"
"v_mfma_f32_32x32x16_bf16 v[16:31], a[152:155], a[24:27], v[16:31]\n"
"v_mfma_f32_32x32x16_bf16 v[32:47], a[156:159], a[24:27], v[32:47]\n"
"s_add_u32 s72, s72, 1024\n"
"s_and_b32 s72, s72, 16383\n"
"buffer_load_dwordx4 a[72:75], v108, s[16:19], s72 offen sc1\n"
"v_mfma_f32_32x32x16_bf16 v[48:63], a[152:155], a[28:31], v[48:63]\n"
"v_mfma_f32_32x32x16_bf16 v[64:79], a[156:159], a[28:31], v[64:79]\n"
"buffer_load_dwordx4 a[76:79], v109, s[16:19], s72 offen sc1\n"
"v_mfma_f32_32x32x16_bf16 v[16:31], a[160:163], a[32:35], v[16:31]\n"
"v_mfma_f32_32x32x16_bf16 v[32:47], a[164:167], a[32:35], v[32:47]\n"
"s_add_u32 s72, s72, 1024\n"
"s_and_b32 s72, s72, 16383\n"
"buffer_load_dwordx4 a[80:83], v108, s[16:19], s72 offen sc1\n"
"v_mfma_f32_32x32x16_bf16 v[48:63], a[160:163], a[36:39], v[48:63]\n"
"s_waitcnt vmcnt(5)\n"
"s_mov_b64 exec, 1\n"
"buffer_store_dword v9, off, s[16:19], s6 offset:8 sc1\n"
"s_mov_b64 exec, -1\n"
"v_mfma_f32_32x32x16_bf16 v[64:79], a[164:167], a[36:39], v[64:79]\n"
"buffer_load_dwordx4 a[84:87], v109, s[16:19], s72 offen sc1\n"
"v_mfma_f32_32x32x16_bf16 v[16:31], a[168:171], a[40:43], v[16:31]\n"
"s_add_u32 s72, s72, 1024\n"
"s_and_b32 s72, s72, 16383\n"
"buffer_load_dwordx4 a[88:91], v108, s[16:19], s72 offen sc1\n"
"v_mfma_f32_32x32x16_bf16 v[32:47], a[172:175], a[40:43], v[32:47]\n"
"v_mfma_f32_32x32x16_bf16 v[48:63], a[168:171], a[44:47], v[48:63]\n"
"buffer_load_dwordx4 a[92:95], v109, s[16:19], s72 offen sc1\n"
"v_mfma_f32_32x32x16_bf16 v[64:79], a[172:175], a[44:47], v[64:79]\n"
"v_mfma_f32_32x32x16_bf16 v[16:31], a[176:179], a[48:51], v[16:31]\n"
"s_add_u32 s72, s72, 1024\n"
"s_and_b32 s72, s72, 16383\n"
"buffer_load_dwordx4 a[96:99], v108, s[16:19], s72 offen sc1\n"
"v_mfma_f32_32x32x16_bf16 v[32:47], a[180:183], a[48:51], v[32:47]\n"
"v_mfma_f32_32x32x16_bf16 v[48:63], a[176:179], a[52:55], v[48:63]\n"
"buffer_load_dwordx4 a[100:103], v109, s[16:19], s72 offen sc1\n"
"v_mfma_f32_32x32x16_bf16 v[64:79], a[180:183], a[52:55], v[64:79]\n"
"v_mfma_f32_32x32x16_bf16 v[16:31], a[184:187], a[56:59], v[16:31]\n"
"s_add_u32 s72, s72, 1024\n"
"s_and_b32 s72, s72, 16383\n"
"buffer_load_dwordx4 a[104:107], v108, s[16:19], s72 offen sc1\n"
"v_mfma_f32_32x32x16_bf16 v[32:47], a[188:191], a[56:59], v[32:47]\n"
"buffer_load_dwordx4 a[108:111], v109, s[16:19], s72 offen sc1\n"
"v_mfma_f32_32x32x16_bf16 v[48:63], a[184:187], a[60:63], v[48:63]\n"
"v_mfma_f32_32x32x16_bf16 v[64:79], a[188:191], a[60:63], v[64:79]\n"
"s_add_u32 s72, s72, 1024\n"
"s_and_b32 s72, s72, 16383\n"
"buffer_load_dwordx4 a[112:115], v108, s[16:19], s72 offen sc1\n"
"s_waitcnt vmcnt(13)\n"
"v_mfma_f32_32x32x16_bf16 v[16:31], a[192:195], a[64:67], v[16:31]\n"
"v_mfma_f32_32x32x16_bf16 v[32:47], a[196:199], a[64:67], v[32:47]\n"
"buffer_load_dwordx4 a[116:119], v109, s[16:19], s72 offen sc1\n"
"s_waitcnt vmcnt(13)\n"
"v_mfma_f32_32x32x16_bf16 v[48:63], a[192:195], a[68:71], v[48:63]\n"
"v_mfma_f32_32x32x16_bf16 v[64:79], a[196:199], a[68:71], v[64:79]\n"
"s_add_u32 s72, s72, 1024\n"
"s_and_b32 s72, s72, 16383\n"
"buffer_load_dwordx4 a[120:123], v108, s[16:19], s72 offen sc1\n"
"s_waitcnt vmcnt(13)\n"
"v_mfma_f32_32x32x16_bf16 v[16:31], a[200:203], a[72:75], v[16:31]\n"
"v_mfma_f32_32x32x16_bf16 v[32:47], a[204:207], a[72:75], v[32:47]\n"
"buffer_load_dwordx4 a[124:127], v109, s[16:19], s72 offen sc1\n"
"s_waitcnt vmcnt(13)\n"
"v_mfma_f32_32x32x16_bf16 v[48:63], a[200:203], a[76:79], v[48:63]\n"
"s_lshl_b32 s72, s70, 10\n"
"s_or_b32 s72, s72, 16384\n"
"buffer_load_dwordx4 a[0:3], v108, s[16:19], s72 offen sc1\n"
"v_mfma_f32_32x32x16_bf16 v[64:79], a[204:207], a[76:79], v[64:79]\n"
"s_waitcnt vmcnt(13)\n"
"v_mfma_f32_32x32x16_bf16 v[16:31], a[208:211], a[80:83], v[16:31]\n"
"s_barrier\n"
"buffer_load_dwordx4 v[112:115], v110, s[16:19], s7 offen sc1\n"
"buffer_load_dwordx4 a[4:7], v109, s[16:19], s72 offen sc1\n"
"v_mfma_f32_32x32x16_bf16 v[32:47], a[212:215], a[80:83], v[32:47]\n"
"s_waitcnt vmcnt(13)\n"
"v_mfma_f32_32x32x16_bf16 v[48:63], a[208:211], a[84:87], v[48:63]\n"
"s_add_u32 s72, s72, 1024\n"
"s_and_b32 s72, s72, 4294959103\n"
"buffer_load_dwordx4 a[8:11], v108, s[16:19], s72 offen sc1\n"
"v_mfma_f32_32x32x16_bf16 v[64:79], a[212:215], a[84:87], v[64:79]\n"
"s_waitcnt vmcnt(13)\n"
"v_mfma_f32_32x32x16_bf16 v[16:31], a[216:219], a[88:91], v[16:31]\n"
"buffer_load_dwordx4 a[12:15], v109, s[16:19], s72 offen sc1\n"
"v_mfma_f32_32x32x16_bf16 v[32:47], a[220:223], a[88:91], v[32:47]\n"
"s_add_u32 s72, s72, 1024\n"
"s_and_b32 s72, s72, 4294959103\n"
"buffer_load_dwordx4 a[16:19], v108, s[16:19], s72 offen sc1\n"
"s_waitcnt vmcnt(14)\n"
"v_mfma_f32_32x32x16_bf16 v[48:63], a[216:219], a[92:95], v[48:63]\n"
"v_mfma_f32_32x32x16_bf16 v[64:79], a[220:223], a[92:95], v[64:79]\n"
"buffer_load_dwordx4 a[20:23], v109, s[16:19], s72 offen sc1\n"
"s_waitcnt vmcnt(14)\n"
"v_mfma_f32_32x32x16_bf16 v[16:31], a[224:227], a[96:99], v[16:31]\n"
"v_mfma_f32_32x32x16_bf16 v[32:47], a[228:231], a[96:99], v[32:47]\n"
"s_add_u32 s72, s72, 1024\n"
"s_and_b32 s72, s72, 4294959103\n"
"buffer_load_dwordx4 a[24:27], v108, s[16:19], s72 offen sc1\n"
"s_waitcnt vmcnt(14)\n"
"v_mfma_f32_32x32x16_bf16 v[48:63], a[224:227], a[100:103], v[48:63]\n"
"v_mfma_f32_32x32x16_bf16 v[64:79], a[228:231], a[100:103], v[64:79]\n"
"buffer_load_dwordx4 a[28:31], v109, s[16:19], s72 offen sc1\n"
"s_waitcnt vmcnt(14)\n"
"v_mfma_f32_32x32x16_bf16 v[16:31], a[232:235], a[104:107], v[16:31]\n"
"v_mfma_f32_32x32x16_bf16 v[32:47], a[236:239], a[104:107], v[32:47]\n"
"s_waitcnt vmcnt(13)\n"
"v_mfma_f32_32x32x16_bf16 v[48:63], a[232:235], a[108:111], v[48:63]\n"
"v_mfma_f32_32x32x16_bf16 v[64:79], a[236:239], a[108:111], v[64:79]\n"
"s_waitcnt vmcnt(12)\n"
"v_mfma_f32_32x32x16_bf16 v[16:31], a[240:243], a[112:115], v[16:31]\n"
"v_mfma_f32_32x32x16_bf16 v[32:47], a[244:247], a[112:115], v[32:47]\n"
"s_waitcnt vmcnt(11)\n"
"v_mfma_f32_32x32x16_bf16 v[48:63], a[240:243], a[116:119], v[48:63]\n"
"s_waitcnt vmcnt(7)\n"
"v_cmp_ne_u32 vcc, s44, v114\n"
"s_cbranch_vccz .LK_lv2ok_14\n"
"s_mov_b32 s53, 32768\n"
".LK_lv2sp_15:\n"
"buffer_load_dwordx4 v[112:115], v110, s[16:19], s7 offen sc1\n"
"s_waitcnt vmcnt(0)\n"
"v_cmp_ne_u32 vcc, s44, v114\n"
"s_cbranch_vccz .LK_lv2ok_14\n"
"s_sub_u32 s53, s53, 1\n"
"s_cbranch_scc0 .LK_lv2sp_15\n"
".LK_lv2ok_14:\n"
"s_mov_b32 s72, 20480\n"
"buffer_load_dwordx4 a[36:39], v109, s[16:19], s72 offen sc1\n"
"v_mfma_f32_32x32x16_bf16 v[64:79], a[244:247], a[116:119], v[64:79]\n"
"s_mov_b32 s72, 21504\n"
"buffer_load_dwordx4 a[44:47], v109, s[16:19], s72 offen sc1\n"
"v_mfma_f32_32x32x16_bf16 v[16:31], a[248:251], a[120:123], v[16:31]\n"
"v_mfma_f32_32x32x16_bf16 v[32:47], a[252:255], a[120:123], v[32:47]\n"
"s_mov_b32 s72, 22528\n"
"buffer_load_dwordx4 a[52:55], v109, s[16:19], s72 offen sc1\n"
"v_mfma_f32_32x32x16_bf16 v[48:63], a[248:251], a[124:127], v[48:63]\n"
"v_mfma_f32_32x32x16_bf16 v[64:79], a[252:255], a[124:127], v[64:79]\n"
"s_mov_b32 s72, 23552\n"
"buffer_load_dwordx4 a[60:63], v109, s[16:19], s72 offen sc1\n"
"v_mfma_f32_32x32x16_bf16 v[16:31], v[160:163], a[0:3], v[16:31]\n"
"v_mfma_f32_32x32x16_bf16 v[32:47], v[164:167], a[0:3], v[32:47]\n"
"s_mov_b32 s72, 24576\n"
"buffer_load_dwordx4 a[68:71], v109, s[16:19], s72 offen sc1\n"
"s_waitcnt vmcnt(11)\n"
"v_mfma_f32_32x32x16_bf16 v[48:63], v[160:163], a[4:7], v[48:63]\n"
"s_mov_b32 s72, 25600\n"
"buffer_load_dwordx4 a[76:79], v109, s[16:19], s72 offen sc1\n"
"v_mfma_f32_32x32x16_bf16 v[64:79], v[164:167], a[4:7], v[64:79]\n"
"s_waitcnt vmcnt(11)\n"
"v_mfma_f32_32x32x16_bf16 v[16:31], v[168:171], a[8:11], v[16:31]\n"
"s_mov_b32 s72, 26624\n"
"buffer_load_dwordx4 a[84:87], v109, s[16:19], s72 offen sc1\n"
"v_mfma_f32_32x32x16_bf16 v[32:47], v[172:175], a[8:11], v[32:47]\n"
"s_waitcnt vmcnt(11)\n"
"v_mfma_f32_32x32x16_bf16 v[48:63], v[168:171], a[12:15], v[48:63]\n"
"s_mov_b32 s72, 27648\n"
"buffer_load_dwordx4 a[92:95], v109, s[16:19], s72 offen sc1\n"
"v_mfma_f32_32x32x16_bf16 v[64:79], v[172:175], a[12:15], v[64:79]\n"
"s_waitcnt vmcnt(11)\n"
"v_mfma_f32_32x32x16_bf16 v[16:31], v[176:179], a[16:19], v[16:31]\n"
"s_mov_b32 s72, 20480\n"
"buffer_load_dwordx4 a[32:35], v108, s[16:19], s72 offen sc1\n"
"v_mfma_f32_32x32x16_bf16 v[32:47], v[180:183], a[16:19], v[32:47]\n"
"s_waitcnt vmcnt(11)\n"
"v_mfma_f32_32x32x16_bf16 v[48:63], v[176:179], a[20:23], v[48:63]\n"
"s_mov_b32 s72, 21504\n"
"buffer_load_dwordx4 a[40:43], v108, s[16:19], s72 offen sc1\n"
"v_mfma_f32_32x32x16_bf16 v[64:79], v[180:183], a[20:23], v[64:79]\n"
"s_mov_b32 s72, 22528\n"
"buffer_load_dwordx4 a[48:51], v108, s[16:19], s72 offen sc1\n"
"s_waitcnt vmcnt(12)\n"
"v_mfma_f32_32x32x16_bf16 v[16:31], v[184:187], a[24:27], v[16:31]\n"
"v_mfma_f32_32x32x16_bf16 v[32:47], v[188:191], a[24:27], v[32:47]\n"
"s_mov_b32 s72, 23552\n"
"buffer_load_dwordx4 a[56:59], v108, s[16:19], s72 offen sc1\n"
"s_waitcnt vmcnt(12)\n"
"v_mfma_f32_32x32x16_bf16 v[48:63], v[184:187], a[28:31], v[48:63]\n"
"v_mfma_f32_32x32x16_bf16 v[64:79], v[188:191], a[28:31], v[64:79]\n"
"s_mov_b32 s72, 24576\n"
"buffer_load_dwordx4 a[64:67], v108, s[16:19], s72 offen sc1\n"
"s_waitcnt vmcnt(12)\n"
"v_mfma_f32_32x32x16_bf16 v[64:79], v[196:199], a[36:39], v[64:79]\n"
"s_waitcnt vmcnt(11)\n"
"v_mfma_f32_32x32x16_bf16 v[64:79], v[204:207], a[44:47], v[64:79]\n"
"s_mov_b32 s72, 25600\n"
"buffer_load_dwordx4 a[72:75], v108, s[16:19], s72 offen sc1\n"
"s_waitcnt vmcnt(11)\n"
"v_mfma_f32_32x32x16_bf16 v[64:79], v[212:215], a[52:55], v[64:79]\n"
"s_waitcnt vmcnt(10)\n"
"v_mfma_f32_32x32x16_bf16 v[64:79], v[220:223], a[60:63], v[64:79]\n"
"s_mov_b32 s72, 26624\n"
"buffer_load_dwordx4 a[80:83], v108, s[16:19], s72 offen sc1\n"
"s_waitcnt vmcnt(10)\n"
"v_mfma_f32_32x32x16_bf16 v[64:79], v[228:231], a[68:71], v[64:79]\n"
"s_mov_b32 s72, 27648\n"
"buffer_load_dwordx4 a[88:91], v108, s[16:19], s72 offen sc1\n"
"s_waitcnt vmcnt(10)\n"
"v_mfma_f32_32x32x16_bf16 v[64:79], v[236:239], a[76:79], v[64:79]\n"
"s_waitcnt vmcnt(9)\n"
"v_mfma_f32_32x32x16_bf16 v[64:79], v[244:247], a[84:87], v[64:79]\n"
"s_waitcnt vmcnt(8)\n"
"v_mfma_f32_32x32x16_bf16 v[64:79], v[252:255], a[92:95], v[64:79]\n"
"v_mfma_f32_32x32x16_bf16 v[48:63], v[192:195], a[36:39], v[48:63]\n"
"s_nop 4\n"
"v_mfma_f32_32x32x16_bf16 v[48:63], v[200:203], a[44:47], v[48:63]\n"
"s_nop 4\n"
"v_mfma_f32_32x32x16_bf16 v[48:63], v[208:211], a[52:55], v[48:63]\n"
"ds_write_b128 v118, v[64:67] offset:0\n"
"v_mfma_f32_32x32x16_bf16 v[48:63], v[216:219], a[60:63], v[48:63]\n"
"ds_write_b128 v118, v[68:71] offset:1024\n"
"v_mfma_f32_32x32x16_bf16 v[48:63], v[224:227], a[68:71], v[48:63]\n"
"ds_write_b128 v118, v[72:75] offset:2048\n"
"v_mfma_f32_32x32x16_bf16 v[48:63], v[232:235], a[76:79], v[48:63]\n"
"ds_write_b128 v118, v[76:79] offset:3072\n"
"v_mfma_f32_32x32x16_bf16 v[48:63], v[240:243], a[84:87], v[48:63]\n"
"v_mfma_f32_32x32x16_bf16 v[48:63], v[248:251], a[92:95], v[48:63]\n"
"s_waitcnt vmcnt(7)\n"
"v_mfma_f32_32x32x16_bf16 v[32:47], v[196:199], a[32:35], v[32:47]\n"
"s_nop 4\n"
"s_waitcnt vmcnt(6)\n"
"v_mfma_f32_32x32x16_bf16 v[32:47], v[204:207], a[40:43], v[32:47]\n"
"s_nop 4\n"
"s_waitcnt vmcnt(5)\n"
"v_mfma_f32_32x32x16_bf16 v[32:47], v[212:215], a[48:51], v[32:47]\n"
"ds_write_b128 v117, v[48:51] offset:0\n"
"s_waitcnt vmcnt(4)\n"
"v_mfma_f32_32x32x16_bf16 v[32:47], v[220:223], a[56:59], v[32:47]\n"
"ds_write_b128 v117, v[52:55] offset:1024\n"
"s_waitcnt vmcnt(3)\n"
"v_mfma_f32_32x32x16_bf16 v[32:47], v[228:231], a[64:67], v[32:47]\n"
"ds_write_b128 v117, v[56:59] offset:2048\n"
"s_waitcnt vmcnt(2)\n"
"v_mfma_f32_32x32x16_bf16 v[32:47], v[236:239], a[72:75], v[32:47]\n"
"ds_write_b128 v117, v[60:63] offset:3072\n"
"s_waitcnt vmcnt(1)\n"
"v_mfma_f32_32x32x16_bf16 v[32:47], v[244:247], a[80:83], v[32:47]\n"
"s_waitcnt vmcnt(0)\n"
"v_mfma_f32_32x32x16_bf16 v[32:47], v[252:255], a[88:91], v[32:47]\n"
"v_mfma_f32_32x32x16_bf16 v[16:31], v[192:195], a[32:35], v[16:31]\n"
"s_nop 4\n"
"v_mfma_f32_32x32x16_bf16 v[16:31], v[200:203], a[40:43], v[16:31]\n"
"s_nop 4\n"
"v_mfma_f32_32x32x16_bf16 v[16:31], v[208:211], a[48:51], v[16:31]\n"
"ds_write_b128 v116, v[32:35] offset:0\n"
"v_mfma_f32_32x32x16_bf16 v[16:31], v[216:219], a[56:59], v[16:31]\n"
"ds_write_b128 v116, v[36:39] offset:1024\n"
"v_mfma_f32_32x32x16_bf16 v[16:31], v[224:227], a[64:67], v[16:31]\n"
"ds_write_b128 v116, v[40:43] offset:2048\n"
"v_mfma_f32_32x32x16_bf16 v[16:31], v[232:235], a[72:75], v[16:31]\n"
"ds_write_b128 v116, v[44:47] offset:3072\n"
"v_mfma_f32_32x32x16_bf16 v[16:31], v[240:243], a[80:83], v[16:31]\n"
"v_mfma_f32_32x32x16_bf16 v[16:31], v[248:251], a[88:91], v[16:31]\n"
"s_waitcnt lgkmcnt(0)\n"
"s_barrier\n"
"ds_read_b128 v[160:163], v119 offset:0\n"
"ds_read_b128 v[164:167], v119 offset:1024\n"
"ds_read_b128 v[168:171], v119 offset:2048\n"
"ds_read_b128 v[172:175], v119 offset:3072\n"
"ds_read_b128 v[176:179], v119 offset:4096\n"
"ds_read_b128 v[180:183], v119 offset:5120\n"
"ds_read_b128 v[184:187], v119 offset:6144\n"
"ds_read_b128 v[188:191], v119 offset:7168\n"
"ds_read_b128 v[192:195], v119 offset:8192\n"
"ds_read_b128 v[196:199], v119 offset:9216\n"
"ds_read_b128 v[200:203], v119 offset:10240\n"
"ds_read_b128 v[204:207], v119 offset:11264\n"
"v_pk_add_f32 v[144:145], v[16:17], v[144:145]\n"
"v_pk_add_f32 v[146:147], v[18:19], v[146:147]\n"
"v_pk_add_f32 v[148:149], v[20:21], v[148:149]\n"
"v_pk_add_f32 v[150:151], v[22:23], v[150:151]\n"
"v_pk_add_f32 v[152:153], v[24:25], v[152:153]\n"
"v_pk_add_f32 v[154:155], v[26:27], v[154:155]\n"
"v_pk_add_f32 v[156:157], v[28:29], v[156:157]\n"
"v_pk_add_f32 v[158:159], v[30:31], v[158:159]\n"
"s_waitcnt lgkmcnt(11)\n"
"v_pk_add_f32 v[144:145], v[144:145], v[160:161]\n"
"v_pk_add_f32 v[146:147], v[146:147], v[162:163]\n"
"s_waitcnt lgkmcnt(10)\n"
"v_pk_add_f32 v[148:149], v[148:149], v[164:165]\n"
"v_pk_add_f32 v[150:151], v[150:151], v[166:167]\n"
"s_waitcnt lgkmcnt(9)\n"
"v_pk_add_f32 v[152:153], v[152:153], v[168:169]\n"
"v_pk_add_f32 v[154:155], v[154:155], v[170:171]\n"
"s_waitcnt lgkmcnt(8)\n"
"v_pk_add_f32 v[156:157], v[156:157], v[172:173]\n"
"v_pk_add_f32 v[158:159], v[158:159], v[174:175]\n"
"s_waitcnt lgkmcnt(7)\n"
"v_pk_add_f32 v[144:145], v[144:145], v[176:177]\n"
"v_pk_add_f32 v[146:147], v[146:147], v[178:179]\n"
"s_waitcnt lgkmcnt(6)\n"
"v_pk_add_f32 v[148:149], v[148:149], v[180:181]\n"
"v_pk_add_f32 v[150:151], v[150:151], v[182:183]\n"
"s_waitcnt lgkmcnt(5)\n"
"v_pk_add_f32 v[152:153], v[152:153], v[184:185]\n"
"v_pk_add_f32 v[154:155], v[154:155], v[186:187]\n"
"s_waitcnt lgkmcnt(4)\n"
"v_pk_add_f32 v[156:157], v[156:157], v[188:189]\n"
"v_pk_add_f32 v[158:159], v[158:159], v[190:191]\n"
"s_waitcnt lgkmcnt(3)\n"
"v_pk_add_f32 v[144:145], v[144:145], v[192:193]\n"
"v_pk_add_f32 v[146:147], v[146:147], v[194:195]\n"
"s_waitcnt lgkmcnt(2)\n"
"v_pk_add_f32 v[148:149], v[148:149], v[196:197]\n"
"v_pk_add_f32 v[150:151], v[150:151], v[198:199]\n"
"s_waitcnt lgkmcnt(1)\n"
"v_pk_add_f32 v[152:153], v[152:153], v[200:201]\n"
"v_pk_add_f32 v[154:155], v[154:155], v[202:203]\n"
"s_waitcnt lgkmcnt(0)\n"
"v_pk_add_f32 v[156:157], v[156:157], v[204:205]\n"
"v_pk_add_f32 v[158:159], v[158:159], v[206:207]\n"
"v_cvt_pk_bf16_f32 v32, v144, v145\n"
"v_cvt_pk_bf16_f32 v33, v146, v147\n"
"v_cvt_pk_bf16_f32 v34, v148, v149\n"
"v_cvt_pk_bf16_f32 v35, v150, v151\n"
"s_nop 0\n"
"v_permlane32_swap_b32 v32, v34\n"
"v_permlane32_swap_b32 v33, v35\n"
"buffer_store_dwordx4 v[32:35], v10, s[24:27], s76 offen offset:0\n"
"v_cvt_pk_bf16_f32 v36, v152, v153\n"
"v_cvt_pk_bf16_f32 v37, v154, v155\n"
"v_cvt_pk_bf16_f32 v38, v156, v157\n"
"v_cvt_pk_bf16_f32 v39, v158, v159\n"
"s_nop 0\n"
"v_permlane32_swap_b32 v36, v38\n"
"v_permlane32_swap_b32 v37, v39\n"
"buffer_store_dwordx4 v[36:39], v10, s[24:27], s76 offen offset:32\n"
"s_or_b32 s67, s46, s45\n"
"s_cmp_eq_u32 s67, 0\n"
"s_cbranch_scc0 .LK_end_16\n"
"v_mov_b32 v12, s44\n"
"s_mov_b64 exec, 1\n"
"buffer_store_dword v12, off, s[16:19], 0 sc1\n"
"s_mov_b64 exec, -1\n"
".LK_end_16:\n"
".LK_codeend:\n"
  : : "s"(ka), "s"(blockIdx.x), "v"(threadIdx.x), "s"((unsigned)(size_t)lds)
  : "v1", "v2", "v3", "v4", "v5", "v6", "v7", "v8", "v9", "v10", "v11", "v12", "v13", "v14", "v15", "v16", "v17", "v18", "v19", "v20", "v21", "v22", "v23", "v24", "v25", "v26", "v27", "v28", "v29", "v30", "v31", "v32", "v33", "v34", "v35", "v36", "v37", "v38", "v39", "v40", "v41", "v42", "v43", "v44", "v45", "v46", "v47", "v48", "v49", "v50", "v51", "v52", "v53", "v54", "v55", "v56", "v57", "v58", "v59", "v60", "v61", "v62", "v63", "v64", "v65", "v66", "v67", "v68", "v69", "v70", "v71", "v72", "v73", "v74", "v75", "v76", "v77", "v78", "v79", "v80", "v81", "v82", "v83", "v84", "v85", "v86", "v87", "v88", "v89", "v90", "v91", "v92", "v93", "v94", "v95", "v96", "v97", "v98", "v99", "v100", "v101", "v102", "v103", "v104", "v105", "v106", "v107", "v108", "v109", "v110", "v111", "v112", "v113", "v114", "v115", "v116", "v117", "v118", "v119", "v120", "v121", "v122", "v123", "v124", "v125", "v126", "v127", "v128", "v129", "v130", "v131", "v132", "v133", "v134", "v135", "v136", "v137", "v138", "v139", "v140", "v141", "v142", "v143", "v144", "v145", "v146", "v147", "v148", "v149", "v150", "v151", "v152", "v153", "v154", "v155", "v156", "v157", "v158", "v159", "v160", "v161", "v162", "v163", "v164", "v165", "v166", "v167", "v168", "v169", "v170", "v171", "v172", "v173", "v174", "v175", "v176", "v177", "v178", "v179", "v180", "v181", "v182", "v183", "v184", "v185", "v186", "v187", "v188", "v189", "v190", "v191", "v192", "v193", "v194", "v195", "v196", "v197", "v198", "v199", "v200", "v201", "v202", "v203", "v204", "v205", "v206", "v207", "v208", "v209", "v210", "v211", "v212", "v213", "v214", "v215", "v216", "v217", "v218", "v219", "v220", "v221", "v222", "v223", "v224", "v225", "v226", "v227", "v228", "v229", "v230", "v231", "v232", "v233", "v234", "v235", "v236", "v237", "v238", "v239", "v240", "v241", "v242", "v243", "v244", "v245", "v246", "v247", "v248", "v249", "v250", "v251", "v252", "v253", "v254", "v255", "a0", "a1", "a2", "a3", "a4", "a5", "a6", "a7", "a8", "a9", "a10", "a11", "a12", "a13", "a14", "a15", "a16", "a17", "a18", "a19", "a20", "a21", "a22", "a23", "a24", "a25", "a26", "a27", "a28", "a29", "a30", "a31", "a32", "a33", "a34", "a35", "a36", "a37", "a38", "a39", "a40", "a41", "a42", "a43", "a44", "a45", "a46", "a47", "a48", "a49", "a50", "a51", "a52", "a53", "a54", "a55", "a56", "a57", "a58", "a59", "a60", "a61", "a62", "a63", "a64", "a65", "a66", "a67", "a68", "a69", "a70", "a71", "a72", "a73", "a74", "a75", "a76", "a77", "a78", "a79", "a80", "a81", "a82", "a83", "a84", "a85", "a86", "a87", "a88", "a89", "a90", "a91", "a92", "a93", "a94", "a95", "a96", "a97", "a98", "a99", "a100", "a101", "a102", "a103", "a104", "a105", "a106", "a107", "a108", "a109", "a110", "a111", "a112", "a113", "a114", "a115", "a116", "a117", "a118", "a119", "a120", "a121", "a122", "a123", "a124", "a125", "a126", "a127", "a128", "a129", "a130", "a131", "a132", "a133", "a134", "a135", "a136", "a137", "a138", "a139", "a140", "a141", "a142", "a143", "a144", "a145", "a146", "a147", "a148", "a149", "a150", "a151", "a152", "a153", "a154", "a155", "a156", "a157", "a158", "a159", "a160", "a161", "a162", "a163", "a164", "a165", "a166", "a167", "a168", "a169", "a170", "a171", "a172", "a173", "a174", "a175", "a176", "a177", "a178", "a179", "a180", "a181", "a182", "a183", "a184", "a185", "a186", "a187", "a188", "a189", "a190", "a191", "a192", "a193", "a194", "a195", "a196", "a197", "a198", "a199", "a200", "a201", "a202", "a203", "a204", "a205", "a206", "a207", "a208", "a209", "a210", "a211", "a212", "a213", "a214", "a215", "a216", "a217", "a218", "a219", "a220", "a221", "a222", "a223", "a224", "a225", "a226", "a227", "a228", "a229", "a230", "a231", "a232", "a233", "a234", "a235", "a236", "a237", "a238", "a239", "a240", "a241", "a242", "a243", "a244", "a245", "a246", "a247", "a248", "a249", "a250", "a251", "a252", "a253", "a254", "a255", "s4", "s5", "s6", "s7", "s8", "s9", "s10", "s11", "s12", "s13", "s14", "s15", "s16", "s17", "s18", "s19", "s20", "s21", "s22", "s23", "s24", "s25", "s26", "s27", "s28", "s29", "s30", "s31", "s33", "s34", "s35", "s36", "s37", "s38", "s39", "s40", "s41", "s42", "s43", "s44", "s45", "s46", "s47", "s48", "s49", "s50", "s51", "s52", "s53", "s54", "s55", "s56", "s57", "s58", "s59", "s60", "s61", "s62", "s63", "s64", "s65", "s66", "s67", "s68", "s69", "s70", "s71", "s72", "s73", "s74", "s75", "s76", "s77", "s78", "s79", "s80", "s81", "s82", "s83", "s84", "s85", "s86", "s87", "s88", "s89", "s90", "s91", "s92", "s93", "s94", "s95", "s96", "s97", "s98", "s99", "vcc", "scc", "memory");
}
extern "C" void kernel_launch(void* const* d_in, const int* in_sizes, int n_in, void* d_out, int out_size, void* d_ws, size_t ws_size, hipStream_t stream) {
  fused_block_kernel<<<256, 256, 0, stream>>>(d_in[0], d_in[1], d_in[2], d_in[4], d_in[5], d_in[6], d_out, (char*)d_ws + 0);
}
```
